```python
import torch

def _softmax(x, axis=-1):
    return torch.softmax(x, dim=axis)
def _gelu(x):
    return torch.nn.functional.gelu(x, approximate="tanh")
def _silu(x):
    return torch.nn.functional.silu(x)
def _sigmoid(x):
    return torch.sigmoid(x)
def _layernorm(x, axis=-1, eps=1e-5):
    m = x.mean(dim=axis, keepdim=True)
    v = x.var(dim=axis, keepdim=True, unbiased=False)
    return (x - m) / torch.sqrt(v + eps)
def _scatter_add(tgt, idx, src, axis=0):
    return tgt.clone().index_add_(axis, idx, src)
def _segment_sum(src, seg, n):
    out = torch.zeros((n,) + src.shape[1:], dtype=src.dtype)
    return out.index_add_(0, seg, src)
def _pad(x, pw):
    flat = tuple(v for pair in reversed(pw) for v in pair)
    return torch.nn.functional.pad(x, flat)

kernel_name = 'standalone__gqa_softmax__near_ridge'

def setup_inputs():

    _w = (lambda a: (lambda t: t.to(torch.bfloat16) if t.is_floating_point() else t)(torch.as_tensor(a)))
    B, Hq, Hkv, Sq, Sk, D = 8, 32, 32, 384, 512, 192
    return {
        "q": _w((lambda *s: torch.randn(*s, dtype=torch.bfloat16) * 0.8)(B, Hq, Sq, D)),
        "k": _w((lambda *s: torch.randn(*s, dtype=torch.bfloat16) * 0.8)(B, Hkv, Sk, D)),
        "v": _w((lambda *s: torch.randn(*s, dtype=torch.bfloat16) * 0.05)(B, Hkv, Sk, D)),
    }

def reference(q, k, v):
    B, Hq, Sq, D = q.shape
    Hkv = k.shape[1]
    rep = Hq // Hkv
    k = (lambda x, n, axis: torch.repeat_interleave(x, n, dim=axis))(k, rep, axis=1)
    v = (lambda x, n, axis: torch.repeat_interleave(x, n, dim=axis))(v, rep, axis=1)
    scale = D ** -0.5
    scores = torch.einsum("bhqd,bhkd->bhqk", q, k) * scale
    p = _softmax(scores, axis=-1)
    return torch.einsum("bhqk,bhkd->bhqd", p, v)
```

```cpp
#include <hip/hip_runtime.h>
#include <hip/hip_bf16.h>

typedef float    f32x16 __attribute__((ext_vector_type(16)));
typedef float    f32x2  __attribute__((ext_vector_type(2)));
typedef __bf16   bf16x8 __attribute__((ext_vector_type(8)));
typedef __bf16   bf16x2 __attribute__((ext_vector_type(2)));
typedef short    s16x4  __attribute__((ext_vector_type(4)));
typedef short    s16x8  __attribute__((ext_vector_type(8)));
typedef unsigned u32x4  __attribute__((ext_vector_type(4)));
typedef unsigned u32x2  __attribute__((ext_vector_type(2)));

constexpr int NB = 8, NH = 32, SQ = 384, SK = 512, DH = 192;
constexpr int ROWB = DH * 2;
constexpr int LROW = ROWB + 16;

__device__ __forceinline__ unsigned cvtpk(float lo, float hi) {
  f32x2 v = {lo, hi}; bf16x2 b = __builtin_convertvector(v, bf16x2); return __builtin_bit_cast(unsigned, b);
}

__device__ __forceinline__ void exact_block(const char* __restrict__ q, const char* __restrict__ k, const char* __restrict__ v,
                                            char* __restrict__ o, char* lds) {
  const int tid = threadIdx.x, lane = tid & 63, wid = tid >> 6, l32 = lane & 31, hi = lane >> 5;
  const float C = 0.07216878364870322f * 1.4426950408889634f;
  char* Kl = lds; char* Vl = lds + 32 * LROW;
  bf16x8 qf[12];
  const char* qrow = q + (size_t)(wid * 32 + l32) * ROWB + hi * 16;
#pragma unroll
  for (int s = 0; s < 12; ++s) qf[s] = *(const bf16x8*)(qrow + 32 * s);
  f32x16 acc[6];
#pragma unroll
  for (int d = 0; d < 6; ++d)
#pragma unroll
    for (int r = 0; r < 16; ++r) acc[d][r] = 0.f;
  float m = -INFINITY, l = 0.f;
  for (int t = 0; t < SK / 32; ++t) {
    __syncthreads();
#pragma unroll
    for (int i = 0; i < 3; ++i) {
      const int n = tid + 256 * i, row = n / 24, ch = n % 24;
      *(u32x4*)(Kl + row * LROW + ch * 16) = *(const u32x4*)(k + (size_t)(t * 32 + row) * ROWB + ch * 16);
      *(u32x4*)(Vl + row * LROW + ch * 16) = *(const u32x4*)(v + (size_t)(t * 32 + row) * ROWB + ch * 16);
    }
    __syncthreads();
    f32x16 s;
#pragma unroll
    for (int r = 0; r < 16; ++r) s[r] = 0.f;
#pragma unroll
    for (int d0 = 0; d0 < 12; ++d0) {
      const bf16x8 a = *(const bf16x8*)(Kl + l32 * LROW + d0 * 32 + hi * 16);
      s = __builtin_amdgcn_mfma_f32_32x32x16_bf16(a, qf[d0], s, 0, 0, 0);
    }
    float mx = s[0];
#pragma unroll
    for (int r = 1; r < 16; ++r) mx = fmaxf(mx, s[r]);
    mx = fmaxf(mx, __shfl_xor(mx, 32));
    const float mn = fmaxf(m, mx);
    const float mref = (mn == -INFINITY) ? 0.f : mn;
    const float alpha = __builtin_amdgcn_exp2f((m - mref) * C);
    m = mn;
    float ps = 0.f;
#pragma unroll
    for (int r = 0; r < 16; ++r) { s[r] = __builtin_amdgcn_exp2f((s[r] - mref) * C); ps += s[r]; }
    ps += __shfl_xor(ps, 32);
    l = l * alpha + ps;
#pragma unroll
    for (int d = 0; d < 6; ++d)
#pragma unroll
      for (int r = 0; r < 16; ++r) acc[d][r] *= alpha;
#pragma unroll
    for (int ks = 0; ks < 2; ++ks) {
      u32x4 w;
#pragma unroll
      for (int p = 0; p < 4; ++p) w[p] = cvtpk(s[8 * ks + 2 * p], s[8 * ks + 2 * p + 1]);
      const bf16x8 pf = __builtin_bit_cast(bf16x8, w);
      const int g = lane >> 4, a4 = (lane >> 2) & 3, p4 = lane & 3;
      const char* vb = Vl + (16 * ks + 4 * (g >> 1) + a4) * LROW + (16 * (g & 1) + 4 * p4) * 2;
#pragma unroll
      for (int db = 0; db < 6; ++db) {
        auto p0 = (__attribute__((address_space(3))) s16x4*)(vb + db * 64);
        auto p1 = (__attribute__((address_space(3))) s16x4*)(vb + db * 64 + 8 * LROW);
        const s16x4 lo = __builtin_amdgcn_ds_read_tr16_b64_v4i16(p0);
        const s16x4 up = __builtin_amdgcn_ds_read_tr16_b64_v4i16(p1);
        const s16x8 vf = __builtin_shufflevector(lo, up, 0, 1, 2, 3, 4, 5, 6, 7);
        acc[db] = __builtin_amdgcn_mfma_f32_32x32x16_bf16(__builtin_bit_cast(bf16x8, vf), pf, acc[db], 0, 0, 0);
      }
    }
  }
  const float il = 1.0f / l;
  char* orow = o + (size_t)(wid * 32 + l32) * ROWB + hi * 8;
#pragma unroll
  for (int db = 0; db < 6; ++db)
#pragma unroll
    for (int g = 0; g < 4; ++g) {
      u32x2 w;
      w[0] = cvtpk(acc[db][4 * g + 0] * il, acc[db][4 * g + 1] * il);
      w[1] = cvtpk(acc[db][4 * g + 2] * il, acc[db][4 * g + 3] * il);
      *(u32x2*)(orow + db * 64 + g * 16) = w;
    }
}

__global__ __launch_bounds__(256) void attn_base(const char* __restrict__ q, const char* __restrict__ k, const char* __restrict__ v, char* __restrict__ o) {
  __shared__ __attribute__((aligned(16))) char lds[2 * 32 * LROW];
  const int blk = blockIdx.x, head = blk / 3, part = blk % 3;
  exact_block(q + ((size_t)head * SQ + part * 128) * ROWB, k + (size_t)head * SK * ROWB, v + (size_t)head * SK * ROWB,
              o + ((size_t)head * SQ + part * 128) * ROWB, lds);
}

extern "C" void kernel_launch(void* const* d_in, const int* in_sizes, int n_in,
                              void* d_out, int out_size, void* d_ws, size_t ws_size,
                              hipStream_t stream) {
  attn_base<<<NB * NH * 3, 256, 0, stream>>>((const char*)d_in[0], (const char*)d_in[1], (const char*)d_in[2], (char*)d_out);
}
```

```cpp
#include <hip/hip_runtime.h>
#include <hip/hip_bf16.h>

typedef float    f32x16 __attribute__((ext_vector_type(16)));
typedef float    f32x2  __attribute__((ext_vector_type(2)));
typedef __bf16   bf16x8 __attribute__((ext_vector_type(8)));
typedef __bf16   bf16x2 __attribute__((ext_vector_type(2)));
typedef short    s16x4  __attribute__((ext_vector_type(4)));
typedef short    s16x8  __attribute__((ext_vector_type(8)));
typedef unsigned u32x4  __attribute__((ext_vector_type(4)));
typedef unsigned u32x2  __attribute__((ext_vector_type(2)));

constexpr int NHEADS = 256, SQ = 384, SK = 512, DH = 192;
constexpr int ROWB = DH * 2;
constexpr int LDS_BYTES = 163840;
constexpr int BLK = 12288;

__device__ __forceinline__ unsigned cvtpk(float lo, float hi) {
  f32x2 x = {lo, hi}; bf16x2 b = __builtin_convertvector(x, bf16x2); return __builtin_bit_cast(unsigned, b);
}

__device__ __forceinline__ void exact_rows(const char* __restrict__ q, const char* __restrict__ k, const char* __restrict__ v,
                                           char* __restrict__ o, char* lds, int wid) {
  int lane;
  asm volatile("v_mbcnt_lo_u32_b32 %0, -1, 0\n\tv_mbcnt_hi_u32_b32 %0, -1, %0" : "=v"(lane));
  const int l32 = lane & 31, hi = lane >> 5;
  const float C = 0.07216878364870322f * 1.4426950408889634f;
  char* Vl = lds + 2 * BLK + 3072 * wid;
  bf16x8 qf[12];
#pragma unroll
  for (int s = 0; s < 12; ++s) qf[s] = *(const bf16x8*)(q + (size_t)l32 * ROWB + hi * 16 + 32 * s);
  f32x16 acc[6];
#pragma unroll
  for (int d = 0; d < 6; ++d)
#pragma unroll
    for (int r = 0; r < 16; ++r) acc[d][r] = 0.f;
  float m = -INFINITY, l = 0.f;
  for (int t = 0; t < SK / 32; ++t) {
    asm volatile("s_waitcnt lgkmcnt(0)" ::: "memory");
#pragma unroll
    for (int i = 0; i < 12; ++i) {
      const int n = lane + 64 * i, row = n / 24, ch = n % 24;
      *(u32x4*)(Vl + (row >> 3) * BLK + (row & 7) * ROWB + ch * 16) = *(const u32x4*)(v + (size_t)(t * 32 + row) * ROWB + ch * 16);
    }
    f32x16 s;
#pragma unroll
    for (int r = 0; r < 16; ++r) s[r] = 0.f;
#pragma unroll
    for (int d0 = 0; d0 < 12; ++d0) {
      const bf16x8 a = *(const bf16x8*)(k + (size_t)(t * 32 + l32) * ROWB + d0 * 32 + hi * 16);
      s = __builtin_amdgcn_mfma_f32_32x32x16_bf16(a, qf[d0], s, 0, 0, 0);
    }
    float mx = s[0];
#pragma unroll
    for (int r = 1; r < 16; ++r) mx = fmaxf(mx, s[r]);
    mx = fmaxf(mx, __shfl_xor(mx, 32));
    const float mn = fmaxf(m, mx);
    const float mref = (mn == -INFINITY) ? 0.f : mn;
    const float alpha = __builtin_amdgcn_exp2f((m - mref) * C);
    m = mn;
    float ps = 0.f;
#pragma unroll
    for (int r = 0; r < 16; ++r) { s[r] = __builtin_amdgcn_exp2f((s[r] - mref) * C); ps += s[r]; }
    ps += __shfl_xor(ps, 32);
    l = l * alpha + ps;
#pragma unroll
    for (int d = 0; d < 6; ++d)
#pragma unroll
      for (int r = 0; r < 16; ++r) acc[d][r] *= alpha;
    asm volatile("s_waitcnt lgkmcnt(0)" ::: "memory");
#pragma unroll
    for (int ks = 0; ks < 2; ++ks) {
      u32x4 w;
#pragma unroll
      for (int p = 0; p < 4; ++p) w[p] = cvtpk(s[8 * ks + 2 * p], s[8 * ks + 2 * p + 1]);
      const bf16x8 pf = __builtin_bit_cast(bf16x8, w);
      const int g = lane >> 4, a4 = (lane >> 2) & 3, p4 = lane & 3;
      const int r0 = 16 * ks + 4 * (g >> 1) + a4;
      const char* vb = Vl + (r0 >> 3) * BLK + (r0 & 7) * ROWB + (16 * (g & 1) + 4 * p4) * 2;
#pragma unroll
      for (int db = 0; db < 6; ++db) {
        auto p0 = (__attribute__((address_space(3))) s16x4*)(vb + db * 64);
        auto p1 = (__attribute__((address_space(3))) s16x4*)(vb + db * 64 + BLK);
        const s16x4 lo = __builtin_amdgcn_ds_read_tr16_b64_v4i16(p0);
        const s16x4 up = __builtin_amdgcn_ds_read_tr16_b64_v4i16(p1);
        const s16x8 vf = __builtin_shufflevector(lo, up, 0, 1, 2, 3, 4, 5, 6, 7);
        acc[db] = __builtin_amdgcn_mfma_f32_32x32x16_bf16(__builtin_bit_cast(bf16x8, vf), pf, acc[db], 0, 0, 0);
      }
    }
  }
  const float il = 1.0f / l;
  char* orow = o + (size_t)l32 * ROWB + hi * 8;
#pragma unroll
  for (int db = 0; db < 6; ++db)
#pragma unroll
    for (int g = 0; g < 4; ++g) {
      u32x2 w;
      w[0] = cvtpk(acc[db][4 * g + 0] * il, acc[db][4 * g + 1] * il);
      w[1] = cvtpk(acc[db][4 * g + 2] * il, acc[db][4 * g + 3] * il);
      *(u32x2*)(orow + db * 64 + g * 16) = w;
    }
}

__global__ __launch_bounds__(256, 1) void gqa_attn_kernel(const char* __restrict__ q, const char* __restrict__ k, const char* __restrict__ v,
                                                          char* __restrict__ o) {
  extern __shared__ __attribute__((aligned(16))) char lds[];
  const unsigned head = blockIdx.x;
  const unsigned wid = __builtin_amdgcn_readfirstlane(threadIdx.x >> 6);
  const char* qh = q + (size_t)head * (SQ * ROWB);
  const char* kh = k + (size_t)head * (SK * ROWB);
  const char* vh = v + (size_t)head * (SK * ROWB);
  char* oh = o + (size_t)head * (SQ * ROWB);
  unsigned redo;
  asm volatile(
    "s_getreg_b32 s70, hwreg(HW_REG_XCC_ID, 0, 1)\n\t"
    "s_bfe_u32 s71, %6, 0x10003\n\t"
    "s_lshl1_add_u32 s70, s70, s71\n\t"
    "s_lshl_b32 s70, s70, 3\n\t"
    "s_lshr_b32 s70, 0x0c001305, s70\n\t"
    "s_and_b32 s70, s70, 255\n\t"
    "Lstag_%=:\n\t"
    "s_cmp_eq_u32 s70, 0\n\t"
    "s_cbranch_scc1 Lstagd_%=\n\t"
    "s_sleep 4\n\t"
    "s_sub_u32 s70, s70, 1\n\t"
    "s_branch Lstag_%=\n\t"
    "Lstagd_%=:\n\t"
    "s_mov_b64 s[40:41], %2\n\t"
    "s_and_b32 s41, s41, 0xffff\n\t"
    "s_mov_b32 s42, 196608\n\t"
    "s_mov_b32 s43, 0x20000\n\t"
    "s_mov_b64 s[36:37], %1\n\t"
    "s_and_b32 s37, s37, 0xffff\n\t"
    "s_mov_b32 s38, 147456\n\t"
    "s_mov_b32 s39, 0x20000\n\t"
    "s_mov_b32 s52, %5\n\t"
    "s_mul_i32 s53, s52, 3072\n\t"
    "s_mul_i32 s71, s52, 36864\n\t"
    "v_mbcnt_lo_u32_b32 v3, -1, 0\n\t"
    "v_mbcnt_hi_u32_b32 v3, -1, v3\n\t"
    "v_lshrrev_b32 v0, 3, v3\n\t"
    "v_add_u32 v0, 0, v0\n\t"
    "v_mul_u32_u24 v0, 11, v0\n\t"
    "v_lshrrev_b32 v0, 5, v0\n\t"
    "v_and_b32 v0, 6, v0\n\t"
    "v_xor_b32 v0, v3, v0\n\t"
    "v_lshlrev_b32 v16, 4, v0\n\t"
    "v_lshrrev_b32 v0, 3, v3\n\t"
    "v_add_u32 v0, 8, v0\n\t"
    "v_mul_u32_u24 v0, 11, v0\n\t"
    "v_lshrrev_b32 v0, 5, v0\n\t"
    "v_and_b32 v0, 6, v0\n\t"
    "v_xor_b32 v0, v3, v0\n\t"
    "v_lshlrev_b32 v17, 4, v0\n\t"
    "v_lshrrev_b32 v0, 3, v3\n\t"
    "v_add_u32 v0, 16, v0\n\t"
    "v_mul_u32_u24 v0, 11, v0\n\t"
    "v_lshrrev_b32 v0, 5, v0\n\t"
    "v_and_b32 v0, 6, v0\n\t"
    "v_xor_b32 v0, v3, v0\n\t"
    "v_lshlrev_b32 v18, 4, v0\n\t"
    "s_add_u32 s70, s53, 0\n\t"
    "s_add_u32 m0, s53, 147456\n\t"
    "s_nop 0\n\t"
    "buffer_load_dwordx4 v16, s[40:43], s70 offen offset:0 sc1 nt lds\n\t"
    "buffer_load_dwordx4 v17, s[40:43], s70 offen offset:1024 sc1 nt lds\n\t"
    "buffer_load_dwordx4 v18, s[40:43], s70 offen offset:2048 sc1 nt lds\n\t"
    "s_add_u32 s70, s71, 0\n\t"
    "s_add_u32 m0, s53, 0\n\t"
    "s_nop 0\n\t"
    "buffer_load_dwordx4 v16, s[36:39], s70 offen offset:0 sc1 nt lds\n\t"
    "buffer_load_dwordx4 v17, s[36:39], s70 offen offset:1024 sc1 nt lds\n\t"
    "buffer_load_dwordx4 v18, s[36:39], s70 offen offset:2048 sc1 nt lds\n\t"
    "s_add_u32 s70, s71, 3072\n\t"
    "s_add_u32 m0, s53, 12288\n\t"
    "s_nop 0\n\t"
    "buffer_load_dwordx4 v16, s[36:39], s70 offen offset:0 sc1 nt lds\n\t"
    "buffer_load_dwordx4 v17, s[36:39], s70 offen offset:1024 sc1 nt lds\n\t"
    "buffer_load_dwordx4 v18, s[36:39], s70 offen offset:2048 sc1 nt lds\n\t"
    "s_add_u32 s70, s71, 6144\n\t"
    "s_add_u32 m0, s53, 24576\n\t"
    "s_nop 0\n\t"
    "buffer_load_dwordx4 v16, s[36:39], s70 offen offset:0 sc1 nt lds\n\t"
    "buffer_load_dwordx4 v17, s[36:39], s70 offen offset:1024 sc1 nt lds\n\t"
    "buffer_load_dwordx4 v18, s[36:39], s70 offen offset:2048 sc1 nt lds\n\t"
    "s_add_u32 s70, s71, 9216\n\t"
    "s_add_u32 m0, s53, 36864\n\t"
    "s_nop 0\n\t"
    "buffer_load_dwordx4 v16, s[36:39], s70 offen offset:0 sc1 nt lds\n\t"
    "buffer_load_dwordx4 v17, s[36:39], s70 offen offset:1024 sc1 nt lds\n\t"
    "buffer_load_dwordx4 v18, s[36:39], s70 offen offset:2048 sc1 nt lds\n\t"
    "s_getpc_b64 s[72:73]\n\t"
    "Lpc_%=:\n\t"
    "s_and_b32 s73, s73, 0xffff\n\t"
    "s_mov_b32 s74, Lend_%=-Lpc_%=\n\t"
    "s_mov_b32 s75, 0x20000\n\t"
    "s_lshl_b32 s77, s52, 12\n\t"
    "v_lshlrev_b32 v0, 6, v3\n\t"
    "v_add_u32 v0, s77, v0\n\t"
    "buffer_load_dword a112, v0, s[72:75], 0 offen\n\t"
    "v_add_u32 v0, 0x4000, v0\n\t"
    "buffer_load_dword a113, v0, s[72:75], 0 offen\n\t"
    "v_add_u32 v0, 0x4000, v0\n\t"
    "s_mov_b64 s[44:45], %3\n\t"
    "s_and_b32 s45, s45, 0xffff\n\t"
    "s_mov_b32 s46, 196608\n\t"
    "s_mov_b32 s47, 0x20000\n\t"
    "s_mov_b64 s[48:49], %4\n\t"
    "s_and_b32 s49, s49, 0xffff\n\t"
    "s_mov_b32 s50, 147456\n\t"
    "s_mov_b32 s51, 0x20000\n\t"
    "s_mov_b32 s64, 0x3dd53b95\n\t"
    "s_mov_b32 s65, 0x3dd53b95\n\t"
    "s_mov_b32 s66, 0x2b800000\n\t"
    "s_mov_b32 s67, 0x53800000\n\t"
    "s_mov_b32 s76, 0xffff0000\n\t"
    "s_mov_b64 s[68:69], 0\n\t"
    "v_and_b32 v0, 15, v3\n\t"
    "v_lshrrev_b32 v1, 4, v3\n\t"
    "v_and_b32 v2, 2, v0\n\t"
    "v_xor_b32 v2, v1, v2\n\t"
    "v_lshlrev_b32 v2, 4, v2\n\t"
    "v_and_b32 v10, 4, v0\n\t"
    "v_lshlrev_b32 v10, 4, v10\n\t"
    "v_or_b32 v2, v2, v10\n\t"
    "v_mul_u32_u24 v10, 0x180, v0\n\t"
    "v_add_u32 v10, v10, v2\n\t"
    "v_xor_b32 v11, 64, v2\n\t"
    "v_mul_u32_u24 v12, 0x180, v0\n\t"
    "v_add_u32 v11, v11, v12\n\t"
    "v_lshrrev_b32 v92, 3, v0\n\t"
    "v_mul_u32_u24 v92, 0x3000, v92\n\t"
    "v_and_b32 v93, 7, v0\n\t"
    "v_mul_u32_u24 v93, 0x180, v93\n\t"
    "v_add_u32 v92, v92, v93\n\t"
    "v_add_u32 v92, s53, v92\n\t"
    "v_xor_b32 v93, 64, v2\n\t"
    "v_add_u32 v93, v92, v93\n\t"
    "v_add_u32 v92, v92, v2\n\t"
    "v_lshlrev_b32 v19, 4, v3\n\t"
    "v_add_u32 v19, s53, v19\n\t"
    "v_add_u32 v19, 98304, v19\n\t"
    "v_lshrrev_b32 v0, 2, v3\n\t"
    "v_mul_u32_u24 v1, 0x180, v0\n\t"
    "v_and_b32 v2, 3, v3\n\t"
    "v_lshl_add_u32 v1, v2, 3, v1\n\t"
    "v_add_u32 v1, 0x3000, v1\n\t"
    "v_lshrrev_b32 v0, 1, v0\n\t"
    "v_and_b32 v0, 3, v0\n\t"
    "v_xor_b32 v2, 0, v0\n\t"
    "v_lshl_add_u32 v12, v2, 5, v1\n\t"
    "v_xor_b32 v2, 1, v0\n\t"
    "v_lshl_add_u32 v13, v2, 5, v1\n\t"
    "v_xor_b32 v2, 2, v0\n\t"
    "v_lshl_add_u32 v14, v2, 5, v1\n\t"
    "v_xor_b32 v2, 3, v0\n\t"
    "v_lshl_add_u32 v15, v2, 5, v1\n\t"
    "s_mov_b32 s54, 0\n\t"
    "s_mov_b32 s56, 73728\n\t"
    "s_mov_b32 s57, 0\n\t"
    "s_mov_b32 s59, 12288\n\t"
    "s_mov_b32 s61, 12288\n\t"
    "s_mov_b32 s58, 61440\n\t"
    "s_add_u32 s62, s53, 36864\n\t"
    "s_add_u32 s63, s53, 24576\n\t"
    "v_mov_b32 v4, 0\n\t"
    "v_mov_b32 v5, 0\n\t"
    "v_mov_b32 v6, 0\n\t"
    "v_mov_b32 v7, 0\n\t"
    "v_mov_b32 v8, 0\n\t"
    "v_mov_b32 v9, 0\n\t"
    "v_mov_b32 v68, 0\n\t"
    "v_mov_b32 v69, 0\n\t"
    "v_mov_b32 v70, 0\n\t"
    "v_mov_b32 v71, 0\n\t"
    "s_nop 1\n\t"
    "v_mov_b32 v94, 0\n\t"
    "v_mov_b32 v95, 0\n\t"
    "v_mfma_f32_16x16x32_bf16 v[124:127], v[68:71], v[68:71], 0\n\t"
    "v_mfma_f32_16x16x32_bf16 v[128:131], v[68:71], v[68:71], 0\n\t"
    "v_mfma_f32_16x16x32_bf16 v[132:135], v[68:71], v[68:71], 0\n\t"
    "v_mfma_f32_16x16x32_bf16 v[136:139], v[68:71], v[68:71], 0\n\t"
    "v_mfma_f32_16x16x32_bf16 v[140:143], v[68:71], v[68:71], 0\n\t"
    "v_mfma_f32_16x16x32_bf16 v[144:147], v[68:71], v[68:71], 0\n\t"
    "v_mfma_f32_16x16x32_bf16 v[148:151], v[68:71], v[68:71], 0\n\t"
    "v_mfma_f32_16x16x32_bf16 v[152:155], v[68:71], v[68:71], 0\n\t"
    "v_mfma_f32_16x16x32_bf16 v[156:159], v[68:71], v[68:71], 0\n\t"
    "v_mfma_f32_16x16x32_bf16 v[160:163], v[68:71], v[68:71], 0\n\t"
    "v_mfma_f32_16x16x32_bf16 v[164:167], v[68:71], v[68:71], 0\n\t"
    "v_mfma_f32_16x16x32_bf16 v[168:171], v[68:71], v[68:71], 0\n\t"
    "v_mfma_f32_16x16x32_bf16 v[172:175], v[68:71], v[68:71], 0\n\t"
    "v_mfma_f32_16x16x32_bf16 v[176:179], v[68:71], v[68:71], 0\n\t"
    "v_mfma_f32_16x16x32_bf16 v[180:183], v[68:71], v[68:71], 0\n\t"
    "v_mfma_f32_16x16x32_bf16 v[184:187], v[68:71], v[68:71], 0\n\t"
    "v_mfma_f32_16x16x32_bf16 v[188:191], v[68:71], v[68:71], 0\n\t"
    "v_mfma_f32_16x16x32_bf16 v[192:195], v[68:71], v[68:71], 0\n\t"
    "v_mfma_f32_16x16x32_bf16 v[196:199], v[68:71], v[68:71], 0\n\t"
    "v_mfma_f32_16x16x32_bf16 v[200:203], v[68:71], v[68:71], 0\n\t"
    "v_mfma_f32_16x16x32_bf16 v[204:207], v[68:71], v[68:71], 0\n\t"
    "v_mfma_f32_16x16x32_bf16 v[208:211], v[68:71], v[68:71], 0\n\t"
    "v_mfma_f32_16x16x32_bf16 v[212:215], v[68:71], v[68:71], 0\n\t"
    "v_mfma_f32_16x16x32_bf16 v[216:219], v[68:71], v[68:71], 0\n\t"
    "v_mfma_f32_16x16x32_bf16 v[220:223], v[68:71], v[68:71], 0\n\t"
    "v_mfma_f32_16x16x32_bf16 v[224:227], v[68:71], v[68:71], 0\n\t"
    "v_mfma_f32_16x16x32_bf16 v[228:231], v[68:71], v[68:71], 0\n\t"
    "v_mfma_f32_16x16x32_bf16 v[232:235], v[68:71], v[68:71], 0\n\t"
    "v_mfma_f32_16x16x32_bf16 v[236:239], v[68:71], v[68:71], 0\n\t"
    "v_mfma_f32_16x16x32_bf16 v[240:243], v[68:71], v[68:71], 0\n\t"
    "v_mfma_f32_16x16x32_bf16 v[244:247], v[68:71], v[68:71], 0\n\t"
    "v_mfma_f32_16x16x32_bf16 v[248:251], v[68:71], v[68:71], 0\n\t"
    "v_mfma_f32_16x16x32_bf16 v[252:255], v[68:71], v[68:71], 0\n\t"
    "v_mfma_f32_16x16x32_bf16 a[180:183], v[68:71], v[68:71], 0\n\t"
    "v_mfma_f32_16x16x32_bf16 a[184:187], v[68:71], v[68:71], 0\n\t"
    "v_mfma_f32_16x16x32_bf16 a[188:191], v[68:71], v[68:71], 0\n\t"
    "v_mfma_f32_16x16x32_bf16 a[192:195], v[68:71], v[68:71], 0\n\t"
    "v_mfma_f32_16x16x32_bf16 a[196:199], v[68:71], v[68:71], 0\n\t"
    "v_mfma_f32_16x16x32_bf16 a[200:203], v[68:71], v[68:71], 0\n\t"
    "v_mfma_f32_16x16x32_bf16 a[204:207], v[68:71], v[68:71], 0\n\t"
    "v_mfma_f32_16x16x32_bf16 a[208:211], v[68:71], v[68:71], 0\n\t"
    "v_mfma_f32_16x16x32_bf16 a[212:215], v[68:71], v[68:71], 0\n\t"
    "v_mfma_f32_16x16x32_bf16 a[216:219], v[68:71], v[68:71], 0\n\t"
    "v_mfma_f32_16x16x32_bf16 a[220:223], v[68:71], v[68:71], 0\n\t"
    "v_mfma_f32_16x16x32_bf16 a[224:227], v[68:71], v[68:71], 0\n\t"
    "v_mfma_f32_16x16x32_bf16 a[228:231], v[68:71], v[68:71], 0\n\t"
    "v_mfma_f32_16x16x32_bf16 a[232:235], v[68:71], v[68:71], 0\n\t"
    "v_mfma_f32_16x16x32_bf16 a[236:239], v[68:71], v[68:71], 0\n\t"
    "v_mfma_f32_16x16x32_bf16 a[240:243], v[68:71], v[68:71], 0\n\t"
    "v_mfma_f32_16x16x32_bf16 a[244:247], v[68:71], v[68:71], 0\n\t"
    "v_mfma_f32_16x16x32_bf16 a[248:251], v[68:71], v[68:71], 0\n\t"
    "v_mfma_f32_16x16x32_bf16 a[252:255], v[68:71], v[68:71], 0\n\t"
    "s_waitcnt vmcnt(14)\n\t"
    "s_barrier\n\t"
    "v_add_u32 v0, 147456, v10\n\t"
    "v_add_u32 v1, 147456, v11\n\t"
    "ds_read_b128 a[132:135], v0 offset:0\n\t"
    "ds_read_b128 a[136:139], v1 offset:0\n\t"
    "ds_read_b128 a[140:143], v0 offset:128\n\t"
    "ds_read_b128 a[144:147], v1 offset:128\n\t"
    "ds_read_b128 a[148:151], v0 offset:256\n\t"
    "ds_read_b128 a[152:155], v1 offset:256\n\t"
    "ds_read_b128 a[156:159], v0 offset:6144\n\t"
    "ds_read_b128 a[160:163], v1 offset:6144\n\t"
    "ds_read_b128 a[164:167], v0 offset:6272\n\t"
    "ds_read_b128 a[168:171], v1 offset:6272\n\t"
    "ds_read_b128 a[172:175], v0 offset:6400\n\t"
    "ds_read_b128 a[176:179], v1 offset:6400\n\t"
    "v_mov_b32 v47, v3\n\t"
    "v_bfe_u32 v44, v47, 3, 1\n\t"
    "v_mul_u32_u24 v46, 0x3000, v44\n\t"
    "v_and_b32 v44, 7, v47\n\t"
    "v_mul_u32_u24 v45, 0xc0, v44\n\t"
    "v_add_u32 v46, v46, v45\n\t"
    "v_bfe_u32 v45, v47, 4, 1\n\t"
    "v_mul_u32_u24 v45, 0x600, v45\n\t"
    "v_add_u32 v46, v46, v45\n\t"
    "v_lshrrev_b32 v44, 1, v44\n\t"
    "v_lshrrev_b32 v45, 5, v47\n\t"
    "v_xor_b32 v44, v44, v45\n\t"
    "v_lshl_add_u32 v46, v44, 4, v46\n\t"
    "s_add_u32 s70, s53, 24576\n\t"
    "v_add_u32 v0, s70, v46\n\t"
    "v_lshrrev_b32 v44, 3, v47\n\t"
    "v_lshlrev_b32 v46, 6, v44\n\t"
    "v_and_b32 v45, 2, v47\n\t"
    "v_lshl_add_u32 v46, v45, 2, v46\n\t"
    "v_add_u32 v46, s70, v46\n\t"
    "v_bfe_u32 v45, v47, 2, 1\n\t"
    "v_and_b32 v47, 1, v47\n\t"
    "v_lshl_or_b32 v47, v45, 1, v47\n\t"
    "v_add_u32 v45, 0, v44\n\t"
    "v_mul_u32_u24 v45, 11, v45\n\t"
    "v_bfe_u32 v45, v45, 6, 2\n\t"
    "v_xor_b32 v45, v45, v47\n\t"
    "v_lshl_add_u32 v1, v45, 4, v46\n\t"
    "v_add_u32 v45, 8, v44\n\t"
    "v_mul_u32_u24 v45, 11, v45\n\t"
    "v_bfe_u32 v45, v45, 6, 2\n\t"
    "v_xor_b32 v45, v45, v47\n\t"
    "v_lshl_add_u32 v2, v45, 4, v46\n\t"
    "v_add_u32 v45, 16, v44\n\t"
    "v_mul_u32_u24 v45, 11, v45\n\t"
    "v_bfe_u32 v45, v45, 6, 2\n\t"
    "v_xor_b32 v45, v45, v47\n\t"
    "v_lshl_add_u32 v3, v45, 4, v46\n\t"
    "s_waitcnt vmcnt(8)\n\t"
    "ds_read_b128 v[44:47], v92 offset:0\n\t"
    "ds_read_b128 v[48:51], v93 offset:0\n\t"
    "ds_read_b128 v[52:55], v92 offset:128\n\t"
    "ds_read_b128 v[56:59], v93 offset:128\n\t"
    "ds_read_b128 v[60:63], v92 offset:256\n\t"
    "ds_read_b128 v[64:67], v93 offset:256\n\t"
    "v_add_u32 v92, 0x6000, v92\n\t"
    "v_add_u32 v93, 0x6000, v93\n\t"
    "s_waitcnt lgkmcnt(5)\n\t"
    "v_lshlrev_b32 v120, 16, v44\n\t"
    "v_and_b32 v121, s76, v44\n\t"
    "v_lshlrev_b32 v122, 16, v45\n\t"
    "v_and_b32 v123, s76, v45\n\t"
    "v_pk_mul_f32 v[120:121], v[120:121], s[64:65]\n\t"
    "v_pk_mul_f32 v[122:123], v[122:123], s[64:65]\n\t"
    "v_cvt_pk_bf16_f32 v44, v120, v121\n\t"
    "v_cvt_pk_bf16_f32 v45, v122, v123\n\t"
    "v_lshlrev_b32 v120, 16, v46\n\t"
    "v_and_b32 v121, s76, v46\n\t"
    "v_lshlrev_b32 v122, 16, v47\n\t"
    "v_and_b32 v123, s76, v47\n\t"
    "v_pk_mul_f32 v[120:121], v[120:121], s[64:65]\n\t"
    "v_pk_mul_f32 v[122:123], v[122:123], s[64:65]\n\t"
    "v_cvt_pk_bf16_f32 v46, v120, v121\n\t"
    "v_cvt_pk_bf16_f32 v47, v122, v123\n\t"
    "s_waitcnt lgkmcnt(4)\n\t"
    "v_lshlrev_b32 v120, 16, v48\n\t"
    "v_and_b32 v121, s76, v48\n\t"
    "s_add_u32 s70, s71, 12288\n\t"
    "s_add_u32 m0, s53, 49152\n\t"
    "s_nop 0\n\t"
    "buffer_load_dwordx4 v16, s[36:39], s70 offen offset:0 sc1 nt lds\n\t"
    "buffer_load_dwordx4 v17, s[36:39], s70 offen offset:1024 sc1 nt lds\n\t"
    "buffer_load_dwordx4 v18, s[36:39], s70 offen offset:2048 sc1 nt lds\n\t"
    "v_lshlrev_b32 v122, 16, v49\n\t"
    "v_mfma_f32_16x16x32_bf16 v[20:23], a[132:135], v[44:47], 0\n\t"
    "v_and_b32 v123, s76, v49\n\t"
    "v_pk_mul_f32 v[120:121], v[120:121], s[64:65]\n\t"
    "v_pk_mul_f32 v[122:123], v[122:123], s[64:65]\n\t"
    "v_cvt_pk_bf16_f32 v48, v120, v121\n\t"
    "v_cvt_pk_bf16_f32 v49, v122, v123\n\t"
    "v_lshlrev_b32 v120, 16, v50\n\t"
    "v_and_b32 v121, s76, v50\n\t"
    "v_lshlrev_b32 v122, 16, v51\n\t"
    "v_mfma_f32_16x16x32_bf16 v[68:71], a[156:159], v[44:47], 0\n\t"
    "v_and_b32 v123, s76, v51\n\t"
    "v_pk_mul_f32 v[120:121], v[120:121], s[64:65]\n\t"
    "v_pk_mul_f32 v[122:123], v[122:123], s[64:65]\n\t"
    "v_cvt_pk_bf16_f32 v50, v120, v121\n\t"
    "v_cvt_pk_bf16_f32 v51, v122, v123\n\t"
    "s_waitcnt lgkmcnt(3)\n\t"
    "v_lshlrev_b32 v120, 16, v52\n\t"
    "v_and_b32 v121, s76, v52\n\t"
    "v_lshlrev_b32 v122, 16, v53\n\t"
    "v_mfma_f32_16x16x32_bf16 v[20:23], a[136:139], v[48:51], v[20:23]\n\t"
    "v_and_b32 v123, s76, v53\n\t"
    "v_pk_mul_f32 v[120:121], v[120:121], s[64:65]\n\t"
    "v_pk_mul_f32 v[122:123], v[122:123], s[64:65]\n\t"
    "v_cvt_pk_bf16_f32 v52, v120, v121\n\t"
    "v_cvt_pk_bf16_f32 v53, v122, v123\n\t"
    "v_lshlrev_b32 v120, 16, v54\n\t"
    "v_and_b32 v121, s76, v54\n\t"
    "v_lshlrev_b32 v122, 16, v55\n\t"
    "v_mfma_f32_16x16x32_bf16 v[68:71], a[160:163], v[48:51], v[68:71]\n\t"
    "v_and_b32 v123, s76, v55\n\t"
    "v_pk_mul_f32 v[120:121], v[120:121], s[64:65]\n\t"
    "v_pk_mul_f32 v[122:123], v[122:123], s[64:65]\n\t"
    "v_cvt_pk_bf16_f32 v54, v120, v121\n\t"
    "v_cvt_pk_bf16_f32 v55, v122, v123\n\t"
    "s_waitcnt lgkmcnt(2)\n\t"
    "v_lshlrev_b32 v120, 16, v56\n\t"
    "v_and_b32 v121, s76, v56\n\t"
    "v_lshlrev_b32 v122, 16, v57\n\t"
    "v_mfma_f32_16x16x32_bf16 v[20:23], a[140:143], v[52:55], v[20:23]\n\t"
    "v_and_b32 v123, s76, v57\n\t"
    "v_pk_mul_f32 v[120:121], v[120:121], s[64:65]\n\t"
    "v_pk_mul_f32 v[122:123], v[122:123], s[64:65]\n\t"
    "v_cvt_pk_bf16_f32 v56, v120, v121\n\t"
    "v_cvt_pk_bf16_f32 v57, v122, v123\n\t"
    "v_lshlrev_b32 v120, 16, v58\n\t"
    "s_add_u32 s70, s71, 15360\n\t"
    "s_add_u32 m0, s53, 61440\n\t"
    "s_nop 0\n\t"
    "buffer_load_dwordx4 v16, s[36:39], s70 offen offset:0 sc1 nt lds\n\t"
    "buffer_load_dwordx4 v17, s[36:39], s70 offen offset:1024 sc1 nt lds\n\t"
    "buffer_load_dwordx4 v18, s[36:39], s70 offen offset:2048 sc1 nt lds\n\t"
    "v_and_b32 v121, s76, v58\n\t"
    "v_lshlrev_b32 v122, 16, v59\n\t"
    "v_mfma_f32_16x16x32_bf16 v[68:71], a[164:167], v[52:55], v[68:71]\n\t"
    "v_and_b32 v123, s76, v59\n\t"
    "v_pk_mul_f32 v[120:121], v[120:121], s[64:65]\n\t"
    "v_pk_mul_f32 v[122:123], v[122:123], s[64:65]\n\t"
    "v_cvt_pk_bf16_f32 v58, v120, v121\n\t"
    "v_cvt_pk_bf16_f32 v59, v122, v123\n\t"
    "s_waitcnt lgkmcnt(1)\n\t"
    "v_lshlrev_b32 v120, 16, v60\n\t"
    "v_and_b32 v121, s76, v60\n\t"
    "v_lshlrev_b32 v122, 16, v61\n\t"
    "v_mfma_f32_16x16x32_bf16 v[20:23], a[144:147], v[56:59], v[20:23]\n\t"
    "v_and_b32 v123, s76, v61\n\t"
    "v_pk_mul_f32 v[120:121], v[120:121], s[64:65]\n\t"
    "v_pk_mul_f32 v[122:123], v[122:123], s[64:65]\n\t"
    "v_cvt_pk_bf16_f32 v60, v120, v121\n\t"
    "v_cvt_pk_bf16_f32 v61, v122, v123\n\t"
    "v_lshlrev_b32 v120, 16, v62\n\t"
    "v_and_b32 v121, s76, v62\n\t"
    "v_lshlrev_b32 v122, 16, v63\n\t"
    "v_mfma_f32_16x16x32_bf16 v[68:71], a[168:171], v[56:59], v[68:71]\n\t"
    "v_and_b32 v123, s76, v63\n\t"
    "v_pk_mul_f32 v[120:121], v[120:121], s[64:65]\n\t"
    "v_pk_mul_f32 v[122:123], v[122:123], s[64:65]\n\t"
    "v_cvt_pk_bf16_f32 v62, v120, v121\n\t"
    "v_cvt_pk_bf16_f32 v63, v122, v123\n\t"
    "s_waitcnt lgkmcnt(0)\n\t"
    "v_lshlrev_b32 v120, 16, v64\n\t"
    "v_and_b32 v121, s76, v64\n\t"
    "v_lshlrev_b32 v122, 16, v65\n\t"
    "v_mfma_f32_16x16x32_bf16 v[20:23], a[148:151], v[60:63], v[20:23]\n\t"
    "v_and_b32 v123, s76, v65\n\t"
    "v_pk_mul_f32 v[120:121], v[120:121], s[64:65]\n\t"
    "v_pk_mul_f32 v[122:123], v[122:123], s[64:65]\n\t"
    "v_cvt_pk_bf16_f32 v64, v120, v121\n\t"
    "v_cvt_pk_bf16_f32 v65, v122, v123\n\t"
    "v_lshlrev_b32 v120, 16, v66\n\t"
    "v_and_b32 v121, s76, v66\n\t"
    "v_lshlrev_b32 v122, 16, v67\n\t"
    "v_mfma_f32_16x16x32_bf16 v[68:71], a[172:175], v[60:63], v[68:71]\n\t"
    "v_and_b32 v123, s76, v67\n\t"
    "v_pk_mul_f32 v[120:121], v[120:121], s[64:65]\n\t"
    "v_pk_mul_f32 v[122:123], v[122:123], s[64:65]\n\t"
    "v_cvt_pk_bf16_f32 v66, v120, v121\n\t"
    "v_cvt_pk_bf16_f32 v67, v122, v123\n\t"
    "s_nop 1\n\t"
    "v_mfma_f32_16x16x32_bf16 v[20:23], a[152:155], v[64:67], v[20:23]\n\t"
    "v_mfma_f32_16x16x32_bf16 v[68:71], a[176:179], v[64:67], v[68:71]\n\t"
    "s_waitcnt vmcnt(8)\n\t"
    "ds_read_b128 v[96:99], v92 offset:0\n\t"
    "ds_read_b128 v[100:103], v93 offset:0\n\t"
    "ds_read_b128 v[104:107], v92 offset:128\n\t"
    "ds_read_b128 v[108:111], v93 offset:128\n\t"
    "ds_read_b128 v[112:115], v92 offset:256\n\t"
    "ds_read_b128 v[116:119], v93 offset:256\n\t"
    "v_add_u32 v92, 0x6000, v92\n\t"
    "v_add_u32 v93, 0x6000, v93\n\t"
    "s_add_u32 s70, s71, 18432\n\t"
    "s_add_u32 m0, s53, 73728\n\t"
    "s_nop 0\n\t"
    "buffer_load_dwordx4 v16, s[36:39], s70 offen offset:0 sc1 nt lds\n\t"
    "buffer_load_dwordx4 v17, s[36:39], s70 offen offset:1024 sc1 nt lds\n\t"
    "buffer_load_dwordx4 v18, s[36:39], s70 offen offset:2048 sc1 nt lds\n\t"
    "v_accvgpr_write_b32 a0, v44\n\t"
    "v_accvgpr_write_b32 a1, v45\n\t"
    "v_accvgpr_write_b32 a2, v46\n\t"
    "v_accvgpr_write_b32 a3, v47\n\t"
    "v_accvgpr_write_b32 a4, v48\n\t"
    "v_accvgpr_write_b32 a5, v49\n\t"
    "v_accvgpr_write_b32 a6, v50\n\t"
    "v_accvgpr_write_b32 a7, v51\n\t"
    "v_accvgpr_write_b32 a8, v52\n\t"
    "v_accvgpr_write_b32 a9, v53\n\t"
    "v_accvgpr_write_b32 a10, v54\n\t"
    "v_accvgpr_write_b32 a11, v55\n\t"
    "v_accvgpr_write_b32 a12, v56\n\t"
    "v_accvgpr_write_b32 a13, v57\n\t"
    "v_accvgpr_write_b32 a14, v58\n\t"
    "v_accvgpr_write_b32 a15, v59\n\t"
    "v_accvgpr_write_b32 a16, v60\n\t"
    "v_accvgpr_write_b32 a17, v61\n\t"
    "v_accvgpr_write_b32 a18, v62\n\t"
    "v_accvgpr_write_b32 a19, v63\n\t"
    "v_accvgpr_write_b32 a20, v64\n\t"
    "v_accvgpr_write_b32 a21, v65\n\t"
    "v_accvgpr_write_b32 a22, v66\n\t"
    "v_accvgpr_write_b32 a23, v67\n\t"
    "v_exp_f32 v20, v20\n\t"
    "v_exp_f32 v21, v21\n\t"
    "s_nop 0\n\t"
    "v_add_f32 v4, v4, v20\n\t"
    "v_add_f32 v4, v4, v21\n\t"
    "v_cvt_pk_bf16_f32 v20, v20, v21\n\t"
    "v_exp_f32 v22, v22\n\t"
    "v_exp_f32 v23, v23\n\t"
    "s_nop 0\n\t"
    "v_add_f32 v4, v4, v22\n\t"
    "v_add_f32 v4, v4, v23\n\t"
    "v_cvt_pk_bf16_f32 v21, v22, v23\n\t"
    "v_exp_f32 v68, v68\n\t"
    "v_exp_f32 v69, v69\n\t"
    "s_nop 0\n\t"
    "v_add_f32 v4, v4, v68\n\t"
    "v_add_f32 v4, v4, v69\n\t"
    "v_cvt_pk_bf16_f32 v22, v68, v69\n\t"
    "v_exp_f32 v70, v70\n\t"
    "v_exp_f32 v71, v71\n\t"
    "s_nop 0\n\t"
    "v_add_f32 v4, v4, v70\n\t"
    "v_add_f32 v4, v4, v71\n\t"
    "v_cvt_pk_bf16_f32 v23, v70, v71\n\t"
    "s_waitcnt lgkmcnt(5)\n\t"
    "v_lshlrev_b32 v120, 16, v96\n\t"
    "v_and_b32 v121, s76, v96\n\t"
    "v_lshlrev_b32 v122, 16, v97\n\t"
    "v_and_b32 v123, s76, v97\n\t"
    "v_pk_mul_f32 v[120:121], v[120:121], s[64:65]\n\t"
    "v_pk_mul_f32 v[122:123], v[122:123], s[64:65]\n\t"
    "v_cvt_pk_bf16_f32 v96, v120, v121\n\t"
    "v_cvt_pk_bf16_f32 v97, v122, v123\n\t"
    "v_lshlrev_b32 v120, 16, v98\n\t"
    "v_and_b32 v121, s76, v98\n\t"
    "v_lshlrev_b32 v122, 16, v99\n\t"
    "v_and_b32 v123, s76, v99\n\t"
    "v_pk_mul_f32 v[120:121], v[120:121], s[64:65]\n\t"
    "v_pk_mul_f32 v[122:123], v[122:123], s[64:65]\n\t"
    "v_cvt_pk_bf16_f32 v98, v120, v121\n\t"
    "v_cvt_pk_bf16_f32 v99, v122, v123\n\t"
    "s_waitcnt lgkmcnt(4)\n\t"
    "v_lshlrev_b32 v120, 16, v100\n\t"
    "v_and_b32 v121, s76, v100\n\t"
    "s_add_u32 s70, s71, 21504\n\t"
    "s_add_u32 m0, s53, 86016\n\t"
    "s_nop 0\n\t"
    "buffer_load_dwordx4 v16, s[36:39], s70 offen offset:0 sc1 nt lds\n\t"
    "buffer_load_dwordx4 v17, s[36:39], s70 offen offset:1024 sc1 nt lds\n\t"
    "buffer_load_dwordx4 v18, s[36:39], s70 offen offset:2048 sc1 nt lds\n\t"
    "v_lshlrev_b32 v122, 16, v101\n\t"
    "v_mfma_f32_16x16x32_bf16 v[24:27], a[132:135], v[96:99], 0\n\t"
    "v_and_b32 v123, s76, v101\n\t"
    "v_pk_mul_f32 v[120:121], v[120:121], s[64:65]\n\t"
    "v_pk_mul_f32 v[122:123], v[122:123], s[64:65]\n\t"
    "v_cvt_pk_bf16_f32 v100, v120, v121\n\t"
    "v_cvt_pk_bf16_f32 v101, v122, v123\n\t"
    "v_lshlrev_b32 v120, 16, v102\n\t"
    "v_and_b32 v121, s76, v102\n\t"
    "v_lshlrev_b32 v122, 16, v103\n\t"
    "v_mfma_f32_16x16x32_bf16 v[72:75], a[156:159], v[96:99], 0\n\t"
    "v_and_b32 v123, s76, v103\n\t"
    "v_pk_mul_f32 v[120:121], v[120:121], s[64:65]\n\t"
    "v_pk_mul_f32 v[122:123], v[122:123], s[64:65]\n\t"
    "v_cvt_pk_bf16_f32 v102, v120, v121\n\t"
    "v_cvt_pk_bf16_f32 v103, v122, v123\n\t"
    "s_waitcnt lgkmcnt(3)\n\t"
    "v_lshlrev_b32 v120, 16, v104\n\t"
    "v_and_b32 v121, s76, v104\n\t"
    "v_lshlrev_b32 v122, 16, v105\n\t"
    "v_mfma_f32_16x16x32_bf16 v[24:27], a[136:139], v[100:103], v[24:27]\n\t"
    "v_and_b32 v123, s76, v105\n\t"
    "v_pk_mul_f32 v[120:121], v[120:121], s[64:65]\n\t"
    "v_pk_mul_f32 v[122:123], v[122:123], s[64:65]\n\t"
    "v_cvt_pk_bf16_f32 v104, v120, v121\n\t"
    "v_cvt_pk_bf16_f32 v105, v122, v123\n\t"
    "v_lshlrev_b32 v120, 16, v106\n\t"
    "v_and_b32 v121, s76, v106\n\t"
    "v_lshlrev_b32 v122, 16, v107\n\t"
    "v_mfma_f32_16x16x32_bf16 v[72:75], a[160:163], v[100:103], v[72:75]\n\t"
    "v_and_b32 v123, s76, v107\n\t"
    "v_pk_mul_f32 v[120:121], v[120:121], s[64:65]\n\t"
    "v_pk_mul_f32 v[122:123], v[122:123], s[64:65]\n\t"
    "v_cvt_pk_bf16_f32 v106, v120, v121\n\t"
    "v_cvt_pk_bf16_f32 v107, v122, v123\n\t"
    "s_waitcnt lgkmcnt(2)\n\t"
    "v_lshlrev_b32 v120, 16, v108\n\t"
    "v_and_b32 v121, s76, v108\n\t"
    "v_lshlrev_b32 v122, 16, v109\n\t"
    "v_mfma_f32_16x16x32_bf16 v[24:27], a[140:143], v[104:107], v[24:27]\n\t"
    "v_and_b32 v123, s76, v109\n\t"
    "v_pk_mul_f32 v[120:121], v[120:121], s[64:65]\n\t"
    "v_pk_mul_f32 v[122:123], v[122:123], s[64:65]\n\t"
    "v_cvt_pk_bf16_f32 v108, v120, v121\n\t"
    "v_cvt_pk_bf16_f32 v109, v122, v123\n\t"
    "v_lshlrev_b32 v120, 16, v110\n\t"
    "v_and_b32 v121, s76, v110\n\t"
    "v_lshlrev_b32 v122, 16, v111\n\t"
    "v_mfma_f32_16x16x32_bf16 v[72:75], a[164:167], v[104:107], v[72:75]\n\t"
    "v_and_b32 v123, s76, v111\n\t"
    "v_pk_mul_f32 v[120:121], v[120:121], s[64:65]\n\t"
    "v_pk_mul_f32 v[122:123], v[122:123], s[64:65]\n\t"
    "v_cvt_pk_bf16_f32 v110, v120, v121\n\t"
    "v_cvt_pk_bf16_f32 v111, v122, v123\n\t"
    "s_waitcnt lgkmcnt(1)\n\t"
    "v_lshlrev_b32 v120, 16, v112\n\t"
    "v_and_b32 v121, s76, v112\n\t"
    "v_lshlrev_b32 v122, 16, v113\n\t"
    "v_mfma_f32_16x16x32_bf16 v[24:27], a[144:147], v[108:111], v[24:27]\n\t"
    "v_and_b32 v123, s76, v113\n\t"
    "v_pk_mul_f32 v[120:121], v[120:121], s[64:65]\n\t"
    "v_pk_mul_f32 v[122:123], v[122:123], s[64:65]\n\t"
    "v_cvt_pk_bf16_f32 v112, v120, v121\n\t"
    "v_cvt_pk_bf16_f32 v113, v122, v123\n\t"
    "v_lshlrev_b32 v120, 16, v114\n\t"
    "v_and_b32 v121, s76, v114\n\t"
    "v_lshlrev_b32 v122, 16, v115\n\t"
    "v_mfma_f32_16x16x32_bf16 v[72:75], a[168:171], v[108:111], v[72:75]\n\t"
    "v_and_b32 v123, s76, v115\n\t"
    "v_pk_mul_f32 v[120:121], v[120:121], s[64:65]\n\t"
    "v_pk_mul_f32 v[122:123], v[122:123], s[64:65]\n\t"
    "v_cvt_pk_bf16_f32 v114, v120, v121\n\t"
    "v_cvt_pk_bf16_f32 v115, v122, v123\n\t"
    "s_waitcnt lgkmcnt(0)\n\t"
    "v_lshlrev_b32 v120, 16, v116\n\t"
    "v_and_b32 v121, s76, v116\n\t"
    "v_lshlrev_b32 v122, 16, v117\n\t"
    "v_mfma_f32_16x16x32_bf16 v[24:27], a[148:151], v[112:115], v[24:27]\n\t"
    "v_and_b32 v123, s76, v117\n\t"
    "v_pk_mul_f32 v[120:121], v[120:121], s[64:65]\n\t"
    "v_pk_mul_f32 v[122:123], v[122:123], s[64:65]\n\t"
    "v_cvt_pk_bf16_f32 v116, v120, v121\n\t"
    "v_cvt_pk_bf16_f32 v117, v122, v123\n\t"
    "v_lshlrev_b32 v120, 16, v118\n\t"
    "v_and_b32 v121, s76, v118\n\t"
    "v_lshlrev_b32 v122, 16, v119\n\t"
    "v_mfma_f32_16x16x32_bf16 v[72:75], a[172:175], v[112:115], v[72:75]\n\t"
    "v_and_b32 v123, s76, v119\n\t"
    "v_pk_mul_f32 v[120:121], v[120:121], s[64:65]\n\t"
    "v_pk_mul_f32 v[122:123], v[122:123], s[64:65]\n\t"
    "v_cvt_pk_bf16_f32 v118, v120, v121\n\t"
    "v_cvt_pk_bf16_f32 v119, v122, v123\n\t"
    "s_nop 1\n\t"
    "v_mfma_f32_16x16x32_bf16 v[24:27], a[152:155], v[116:119], v[24:27]\n\t"
    "v_mfma_f32_16x16x32_bf16 v[72:75], a[176:179], v[116:119], v[72:75]\n\t"
    "s_waitcnt vmcnt(6)\n\t"
    "ds_read_b128 v[44:47], v92 offset:0\n\t"
    "ds_read_b128 v[48:51], v93 offset:0\n\t"
    "ds_read_b128 v[52:55], v92 offset:128\n\t"
    "ds_read_b128 v[56:59], v93 offset:128\n\t"
    "ds_read_b128 v[60:63], v92 offset:256\n\t"
    "ds_read_b128 v[64:67], v93 offset:256\n\t"
    "v_add_u32 v92, 0x6000, v92\n\t"
    "v_add_u32 v93, 0x6000, v93\n\t"
    "s_add_u32 s70, s71, 24576\n\t"
    "s_add_u32 m0, s53, 98304\n\t"
    "s_nop 0\n\t"
    "buffer_load_dwordx4 v16, s[36:39], s70 offen offset:0 sc1 nt lds\n\t"
    "buffer_load_dwordx4 v17, s[36:39], s70 offen offset:1024 sc1 nt lds\n\t"
    "buffer_load_dwordx4 v18, s[36:39], s70 offen offset:2048 sc1 nt lds\n\t"
    "v_accvgpr_write_b32 a24, v96\n\t"
    "v_accvgpr_write_b32 a25, v97\n\t"
    "v_accvgpr_write_b32 a26, v98\n\t"
    "v_accvgpr_write_b32 a27, v99\n\t"
    "v_accvgpr_write_b32 a28, v100\n\t"
    "v_accvgpr_write_b32 a29, v101\n\t"
    "v_accvgpr_write_b32 a30, v102\n\t"
    "v_accvgpr_write_b32 a31, v103\n\t"
    "v_accvgpr_write_b32 a32, v104\n\t"
    "v_accvgpr_write_b32 a33, v105\n\t"
    "v_accvgpr_write_b32 a34, v106\n\t"
    "v_accvgpr_write_b32 a35, v107\n\t"
    "v_accvgpr_write_b32 a36, v108\n\t"
    "v_accvgpr_write_b32 a37, v109\n\t"
    "v_accvgpr_write_b32 a38, v110\n\t"
    "v_accvgpr_write_b32 a39, v111\n\t"
    "v_accvgpr_write_b32 a40, v112\n\t"
    "v_accvgpr_write_b32 a41, v113\n\t"
    "v_accvgpr_write_b32 a42, v114\n\t"
    "v_accvgpr_write_b32 a43, v115\n\t"
    "v_accvgpr_write_b32 a44, v116\n\t"
    "v_accvgpr_write_b32 a45, v117\n\t"
    "v_accvgpr_write_b32 a46, v118\n\t"
    "v_accvgpr_write_b32 a47, v119\n\t"
    "v_exp_f32 v24, v24\n\t"
    "v_exp_f32 v25, v25\n\t"
    "s_nop 0\n\t"
    "v_add_f32 v5, v5, v24\n\t"
    "v_add_f32 v5, v5, v25\n\t"
    "v_cvt_pk_bf16_f32 v24, v24, v25\n\t"
    "v_exp_f32 v26, v26\n\t"
    "v_exp_f32 v27, v27\n\t"
    "s_nop 0\n\t"
    "v_add_f32 v5, v5, v26\n\t"
    "v_add_f32 v5, v5, v27\n\t"
    "v_cvt_pk_bf16_f32 v25, v26, v27\n\t"
    "v_exp_f32 v72, v72\n\t"
    "v_exp_f32 v73, v73\n\t"
    "s_nop 0\n\t"
    "v_add_f32 v5, v5, v72\n\t"
    "v_add_f32 v5, v5, v73\n\t"
    "v_cvt_pk_bf16_f32 v26, v72, v73\n\t"
    "v_exp_f32 v74, v74\n\t"
    "v_exp_f32 v75, v75\n\t"
    "s_nop 0\n\t"
    "v_add_f32 v5, v5, v74\n\t"
    "v_add_f32 v5, v5, v75\n\t"
    "v_cvt_pk_bf16_f32 v27, v74, v75\n\t"
    "s_waitcnt lgkmcnt(5)\n\t"
    "v_lshlrev_b32 v120, 16, v44\n\t"
    "v_and_b32 v121, s76, v44\n\t"
    "v_lshlrev_b32 v122, 16, v45\n\t"
    "v_and_b32 v123, s76, v45\n\t"
    "v_pk_mul_f32 v[120:121], v[120:121], s[64:65]\n\t"
    "v_pk_mul_f32 v[122:123], v[122:123], s[64:65]\n\t"
    "v_cvt_pk_bf16_f32 v44, v120, v121\n\t"
    "v_cvt_pk_bf16_f32 v45, v122, v123\n\t"
    "v_lshlrev_b32 v120, 16, v46\n\t"
    "v_and_b32 v121, s76, v46\n\t"
    "v_lshlrev_b32 v122, 16, v47\n\t"
    "v_and_b32 v123, s76, v47\n\t"
    "v_pk_mul_f32 v[120:121], v[120:121], s[64:65]\n\t"
    "v_pk_mul_f32 v[122:123], v[122:123], s[64:65]\n\t"
    "v_cvt_pk_bf16_f32 v46, v120, v121\n\t"
    "v_cvt_pk_bf16_f32 v47, v122, v123\n\t"
    "s_waitcnt lgkmcnt(4)\n\t"
    "v_lshlrev_b32 v120, 16, v48\n\t"
    "v_and_b32 v121, s76, v48\n\t"
    "s_add_u32 s70, s71, 27648\n\t"
    "s_add_u32 m0, s53, 110592\n\t"
    "s_nop 0\n\t"
    "buffer_load_dwordx4 v16, s[36:39], s70 offen offset:0 sc1 nt lds\n\t"
    "buffer_load_dwordx4 v17, s[36:39], s70 offen offset:1024 sc1 nt lds\n\t"
    "buffer_load_dwordx4 v18, s[36:39], s70 offen offset:2048 sc1 nt lds\n\t"
    "v_lshlrev_b32 v122, 16, v49\n\t"
    "v_mfma_f32_16x16x32_bf16 v[28:31], a[132:135], v[44:47], 0\n\t"
    "v_and_b32 v123, s76, v49\n\t"
    "v_pk_mul_f32 v[120:121], v[120:121], s[64:65]\n\t"
    "v_pk_mul_f32 v[122:123], v[122:123], s[64:65]\n\t"
    "v_cvt_pk_bf16_f32 v48, v120, v121\n\t"
    "v_cvt_pk_bf16_f32 v49, v122, v123\n\t"
    "v_lshlrev_b32 v120, 16, v50\n\t"
    "v_and_b32 v121, s76, v50\n\t"
    "v_lshlrev_b32 v122, 16, v51\n\t"
    "v_mfma_f32_16x16x32_bf16 v[76:79], a[156:159], v[44:47], 0\n\t"
    "v_and_b32 v123, s76, v51\n\t"
    "v_pk_mul_f32 v[120:121], v[120:121], s[64:65]\n\t"
    "v_pk_mul_f32 v[122:123], v[122:123], s[64:65]\n\t"
    "v_cvt_pk_bf16_f32 v50, v120, v121\n\t"
    "v_cvt_pk_bf16_f32 v51, v122, v123\n\t"
    "s_waitcnt lgkmcnt(3)\n\t"
    "v_lshlrev_b32 v120, 16, v52\n\t"
    "v_and_b32 v121, s76, v52\n\t"
    "v_lshlrev_b32 v122, 16, v53\n\t"
    "v_mfma_f32_16x16x32_bf16 v[28:31], a[136:139], v[48:51], v[28:31]\n\t"
    "v_and_b32 v123, s76, v53\n\t"
    "v_pk_mul_f32 v[120:121], v[120:121], s[64:65]\n\t"
    "v_pk_mul_f32 v[122:123], v[122:123], s[64:65]\n\t"
    "v_cvt_pk_bf16_f32 v52, v120, v121\n\t"
    "v_cvt_pk_bf16_f32 v53, v122, v123\n\t"
    "v_lshlrev_b32 v120, 16, v54\n\t"
    "v_and_b32 v121, s76, v54\n\t"
    "v_lshlrev_b32 v122, 16, v55\n\t"
    "v_mfma_f32_16x16x32_bf16 v[76:79], a[160:163], v[48:51], v[76:79]\n\t"
    "v_and_b32 v123, s76, v55\n\t"
    "v_pk_mul_f32 v[120:121], v[120:121], s[64:65]\n\t"
    "v_pk_mul_f32 v[122:123], v[122:123], s[64:65]\n\t"
    "v_cvt_pk_bf16_f32 v54, v120, v121\n\t"
    "v_cvt_pk_bf16_f32 v55, v122, v123\n\t"
    "s_waitcnt lgkmcnt(2)\n\t"
    "v_lshlrev_b32 v120, 16, v56\n\t"
    "v_and_b32 v121, s76, v56\n\t"
    "v_lshlrev_b32 v122, 16, v57\n\t"
    "v_mfma_f32_16x16x32_bf16 v[28:31], a[140:143], v[52:55], v[28:31]\n\t"
    "v_and_b32 v123, s76, v57\n\t"
    "v_pk_mul_f32 v[120:121], v[120:121], s[64:65]\n\t"
    "v_pk_mul_f32 v[122:123], v[122:123], s[64:65]\n\t"
    "v_cvt_pk_bf16_f32 v56, v120, v121\n\t"
    "v_cvt_pk_bf16_f32 v57, v122, v123\n\t"
    "v_lshlrev_b32 v120, 16, v58\n\t"
    "v_and_b32 v121, s76, v58\n\t"
    "v_lshlrev_b32 v122, 16, v59\n\t"
    "v_mfma_f32_16x16x32_bf16 v[76:79], a[164:167], v[52:55], v[76:79]\n\t"
    "v_and_b32 v123, s76, v59\n\t"
    "v_pk_mul_f32 v[120:121], v[120:121], s[64:65]\n\t"
    "v_pk_mul_f32 v[122:123], v[122:123], s[64:65]\n\t"
    "v_cvt_pk_bf16_f32 v58, v120, v121\n\t"
    "v_cvt_pk_bf16_f32 v59, v122, v123\n\t"
    "s_waitcnt lgkmcnt(1)\n\t"
    "v_lshlrev_b32 v120, 16, v60\n\t"
    "v_and_b32 v121, s76, v60\n\t"
    "v_lshlrev_b32 v122, 16, v61\n\t"
    "v_mfma_f32_16x16x32_bf16 v[28:31], a[144:147], v[56:59], v[28:31]\n\t"
    "v_and_b32 v123, s76, v61\n\t"
    "v_pk_mul_f32 v[120:121], v[120:121], s[64:65]\n\t"
    "v_pk_mul_f32 v[122:123], v[122:123], s[64:65]\n\t"
    "v_cvt_pk_bf16_f32 v60, v120, v121\n\t"
    "v_cvt_pk_bf16_f32 v61, v122, v123\n\t"
    "v_lshlrev_b32 v120, 16, v62\n\t"
    "v_and_b32 v121, s76, v62\n\t"
    "v_lshlrev_b32 v122, 16, v63\n\t"
    "v_mfma_f32_16x16x32_bf16 v[76:79], a[168:171], v[56:59], v[76:79]\n\t"
    "v_and_b32 v123, s76, v63\n\t"
    "v_pk_mul_f32 v[120:121], v[120:121], s[64:65]\n\t"
    "v_pk_mul_f32 v[122:123], v[122:123], s[64:65]\n\t"
    "v_cvt_pk_bf16_f32 v62, v120, v121\n\t"
    "v_cvt_pk_bf16_f32 v63, v122, v123\n\t"
    "s_waitcnt lgkmcnt(0)\n\t"
    "v_lshlrev_b32 v120, 16, v64\n\t"
    "v_and_b32 v121, s76, v64\n\t"
    "v_lshlrev_b32 v122, 16, v65\n\t"
    "v_mfma_f32_16x16x32_bf16 v[28:31], a[148:151], v[60:63], v[28:31]\n\t"
    "v_and_b32 v123, s76, v65\n\t"
    "v_pk_mul_f32 v[120:121], v[120:121], s[64:65]\n\t"
    "v_pk_mul_f32 v[122:123], v[122:123], s[64:65]\n\t"
    "v_cvt_pk_bf16_f32 v64, v120, v121\n\t"
    "v_cvt_pk_bf16_f32 v65, v122, v123\n\t"
    "v_lshlrev_b32 v120, 16, v66\n\t"
    "v_and_b32 v121, s76, v66\n\t"
    "v_lshlrev_b32 v122, 16, v67\n\t"
    "v_mfma_f32_16x16x32_bf16 v[76:79], a[172:175], v[60:63], v[76:79]\n\t"
    "v_and_b32 v123, s76, v67\n\t"
    "v_pk_mul_f32 v[120:121], v[120:121], s[64:65]\n\t"
    "v_pk_mul_f32 v[122:123], v[122:123], s[64:65]\n\t"
    "v_cvt_pk_bf16_f32 v66, v120, v121\n\t"
    "v_cvt_pk_bf16_f32 v67, v122, v123\n\t"
    "s_nop 1\n\t"
    "v_mfma_f32_16x16x32_bf16 v[28:31], a[152:155], v[64:67], v[28:31]\n\t"
    "v_mfma_f32_16x16x32_bf16 v[76:79], a[176:179], v[64:67], v[76:79]\n\t"
    "s_waitcnt vmcnt(6)\n\t"
    "ds_read_b128 v[96:99], v92 offset:0\n\t"
    "ds_read_b128 v[100:103], v93 offset:0\n\t"
    "ds_read_b128 v[104:107], v92 offset:128\n\t"
    "ds_read_b128 v[108:111], v93 offset:128\n\t"
    "ds_read_b128 v[112:115], v92 offset:256\n\t"
    "ds_read_b128 v[116:119], v93 offset:256\n\t"
    "v_add_u32 v92, 0x6000, v92\n\t"
    "v_add_u32 v93, 0x6000, v93\n\t"
    "s_add_u32 s70, s71, 30720\n\t"
    "s_add_u32 m0, s53, 122880\n\t"
    "s_nop 0\n\t"
    "buffer_load_dwordx4 v16, s[36:39], s70 offen offset:0 sc1 nt lds\n\t"
    "buffer_load_dwordx4 v17, s[36:39], s70 offen offset:1024 sc1 nt lds\n\t"
    "buffer_load_dwordx4 v18, s[36:39], s70 offen offset:2048 sc1 nt lds\n\t"
    "v_accvgpr_write_b32 a48, v44\n\t"
    "v_accvgpr_write_b32 a49, v45\n\t"
    "v_accvgpr_write_b32 a50, v46\n\t"
    "v_accvgpr_write_b32 a51, v47\n\t"
    "v_accvgpr_write_b32 a52, v48\n\t"
    "v_accvgpr_write_b32 a53, v49\n\t"
    "v_accvgpr_write_b32 a54, v50\n\t"
    "v_accvgpr_write_b32 a55, v51\n\t"
    "v_accvgpr_write_b32 a56, v52\n\t"
    "v_accvgpr_write_b32 a57, v53\n\t"
    "v_accvgpr_write_b32 a58, v54\n\t"
    "v_accvgpr_write_b32 a59, v55\n\t"
    "v_accvgpr_write_b32 a60, v56\n\t"
    "v_accvgpr_write_b32 a61, v57\n\t"
    "v_accvgpr_write_b32 a62, v58\n\t"
    "v_accvgpr_write_b32 a63, v59\n\t"
    "v_accvgpr_write_b32 a64, v60\n\t"
    "v_accvgpr_write_b32 a65, v61\n\t"
    "v_accvgpr_write_b32 a66, v62\n\t"
    "v_accvgpr_write_b32 a67, v63\n\t"
    "v_accvgpr_write_b32 a68, v64\n\t"
    "v_accvgpr_write_b32 a69, v65\n\t"
    "v_accvgpr_write_b32 a70, v66\n\t"
    "v_accvgpr_write_b32 a71, v67\n\t"
    "v_exp_f32 v28, v28\n\t"
    "v_exp_f32 v29, v29\n\t"
    "s_nop 0\n\t"
    "v_add_f32 v6, v6, v28\n\t"
    "v_add_f32 v6, v6, v29\n\t"
    "v_cvt_pk_bf16_f32 v28, v28, v29\n\t"
    "v_exp_f32 v30, v30\n\t"
    "v_exp_f32 v31, v31\n\t"
    "s_nop 0\n\t"
    "v_add_f32 v6, v6, v30\n\t"
    "v_add_f32 v6, v6, v31\n\t"
    "v_cvt_pk_bf16_f32 v29, v30, v31\n\t"
    "v_exp_f32 v76, v76\n\t"
    "v_exp_f32 v77, v77\n\t"
    "s_nop 0\n\t"
    "v_add_f32 v6, v6, v76\n\t"
    "v_add_f32 v6, v6, v77\n\t"
    "v_cvt_pk_bf16_f32 v30, v76, v77\n\t"
    "v_exp_f32 v78, v78\n\t"
    "v_exp_f32 v79, v79\n\t"
    "s_nop 0\n\t"
    "v_add_f32 v6, v6, v78\n\t"
    "v_add_f32 v6, v6, v79\n\t"
    "v_cvt_pk_bf16_f32 v31, v78, v79\n\t"
    "s_waitcnt lgkmcnt(5)\n\t"
    "v_lshlrev_b32 v120, 16, v96\n\t"
    "v_and_b32 v121, s76, v96\n\t"
    "v_lshlrev_b32 v122, 16, v97\n\t"
    "v_and_b32 v123, s76, v97\n\t"
    "v_pk_mul_f32 v[120:121], v[120:121], s[64:65]\n\t"
    "v_pk_mul_f32 v[122:123], v[122:123], s[64:65]\n\t"
    "v_cvt_pk_bf16_f32 v96, v120, v121\n\t"
    "v_cvt_pk_bf16_f32 v97, v122, v123\n\t"
    "v_lshlrev_b32 v120, 16, v98\n\t"
    "v_and_b32 v121, s76, v98\n\t"
    "v_lshlrev_b32 v122, 16, v99\n\t"
    "v_and_b32 v123, s76, v99\n\t"
    "v_pk_mul_f32 v[120:121], v[120:121], s[64:65]\n\t"
    "v_pk_mul_f32 v[122:123], v[122:123], s[64:65]\n\t"
    "v_cvt_pk_bf16_f32 v98, v120, v121\n\t"
    "v_cvt_pk_bf16_f32 v99, v122, v123\n\t"
    "s_waitcnt lgkmcnt(4)\n\t"
    "v_lshlrev_b32 v120, 16, v100\n\t"
    "v_and_b32 v121, s76, v100\n\t"
    "s_add_u32 s70, s71, 33792\n\t"
    "s_add_u32 m0, s53, 135168\n\t"
    "s_nop 0\n\t"
    "buffer_load_dwordx4 v16, s[36:39], s70 offen offset:0 sc1 nt lds\n\t"
    "buffer_load_dwordx4 v17, s[36:39], s70 offen offset:1024 sc1 nt lds\n\t"
    "buffer_load_dwordx4 v18, s[36:39], s70 offen offset:2048 sc1 nt lds\n\t"
    "v_lshlrev_b32 v122, 16, v101\n\t"
    "v_mfma_f32_16x16x32_bf16 v[32:35], a[132:135], v[96:99], 0\n\t"
    "v_and_b32 v123, s76, v101\n\t"
    "v_pk_mul_f32 v[120:121], v[120:121], s[64:65]\n\t"
    "v_pk_mul_f32 v[122:123], v[122:123], s[64:65]\n\t"
    "v_cvt_pk_bf16_f32 v100, v120, v121\n\t"
    "v_cvt_pk_bf16_f32 v101, v122, v123\n\t"
    "v_lshlrev_b32 v120, 16, v102\n\t"
    "v_and_b32 v121, s76, v102\n\t"
    "v_lshlrev_b32 v122, 16, v103\n\t"
    "v_mfma_f32_16x16x32_bf16 v[80:83], a[156:159], v[96:99], 0\n\t"
    "v_and_b32 v123, s76, v103\n\t"
    "v_pk_mul_f32 v[120:121], v[120:121], s[64:65]\n\t"
    "v_pk_mul_f32 v[122:123], v[122:123], s[64:65]\n\t"
    "v_cvt_pk_bf16_f32 v102, v120, v121\n\t"
    "v_cvt_pk_bf16_f32 v103, v122, v123\n\t"
    "s_waitcnt lgkmcnt(3)\n\t"
    "v_lshlrev_b32 v120, 16, v104\n\t"
    "v_and_b32 v121, s76, v104\n\t"
    "v_lshlrev_b32 v122, 16, v105\n\t"
    "v_mfma_f32_16x16x32_bf16 v[32:35], a[136:139], v[100:103], v[32:35]\n\t"
    "v_and_b32 v123, s76, v105\n\t"
    "v_pk_mul_f32 v[120:121], v[120:121], s[64:65]\n\t"
    "v_pk_mul_f32 v[122:123], v[122:123], s[64:65]\n\t"
    "v_cvt_pk_bf16_f32 v104, v120, v121\n\t"
    "v_cvt_pk_bf16_f32 v105, v122, v123\n\t"
    "v_lshlrev_b32 v120, 16, v106\n\t"
    "v_and_b32 v121, s76, v106\n\t"
    "v_lshlrev_b32 v122, 16, v107\n\t"
    "v_mfma_f32_16x16x32_bf16 v[80:83], a[160:163], v[100:103], v[80:83]\n\t"
    "v_and_b32 v123, s76, v107\n\t"
    "v_pk_mul_f32 v[120:121], v[120:121], s[64:65]\n\t"
    "v_pk_mul_f32 v[122:123], v[122:123], s[64:65]\n\t"
    "v_cvt_pk_bf16_f32 v106, v120, v121\n\t"
    "v_cvt_pk_bf16_f32 v107, v122, v123\n\t"
    "s_waitcnt lgkmcnt(2)\n\t"
    "v_lshlrev_b32 v120, 16, v108\n\t"
    "v_and_b32 v121, s76, v108\n\t"
    "v_lshlrev_b32 v122, 16, v109\n\t"
    "v_mfma_f32_16x16x32_bf16 v[32:35], a[140:143], v[104:107], v[32:35]\n\t"
    "v_and_b32 v123, s76, v109\n\t"
    "v_pk_mul_f32 v[120:121], v[120:121], s[64:65]\n\t"
    "v_pk_mul_f32 v[122:123], v[122:123], s[64:65]\n\t"
    "v_cvt_pk_bf16_f32 v108, v120, v121\n\t"
    "v_cvt_pk_bf16_f32 v109, v122, v123\n\t"
    "v_lshlrev_b32 v120, 16, v110\n\t"
    "v_and_b32 v121, s76, v110\n\t"
    "v_lshlrev_b32 v122, 16, v111\n\t"
    "v_mfma_f32_16x16x32_bf16 v[80:83], a[164:167], v[104:107], v[80:83]\n\t"
    "v_and_b32 v123, s76, v111\n\t"
    "v_pk_mul_f32 v[120:121], v[120:121], s[64:65]\n\t"
    "v_pk_mul_f32 v[122:123], v[122:123], s[64:65]\n\t"
    "v_cvt_pk_bf16_f32 v110, v120, v121\n\t"
    "v_cvt_pk_bf16_f32 v111, v122, v123\n\t"
    "s_waitcnt lgkmcnt(1)\n\t"
    "v_lshlrev_b32 v120, 16, v112\n\t"
    "v_and_b32 v121, s76, v112\n\t"
    "v_lshlrev_b32 v122, 16, v113\n\t"
    "v_mfma_f32_16x16x32_bf16 v[32:35], a[144:147], v[108:111], v[32:35]\n\t"
    "v_and_b32 v123, s76, v113\n\t"
    "v_pk_mul_f32 v[120:121], v[120:121], s[64:65]\n\t"
    "v_pk_mul_f32 v[122:123], v[122:123], s[64:65]\n\t"
    "v_cvt_pk_bf16_f32 v112, v120, v121\n\t"
    "v_cvt_pk_bf16_f32 v113, v122, v123\n\t"
    "v_lshlrev_b32 v120, 16, v114\n\t"
    "v_and_b32 v121, s76, v114\n\t"
    "v_lshlrev_b32 v122, 16, v115\n\t"
    "v_mfma_f32_16x16x32_bf16 v[80:83], a[168:171], v[108:111], v[80:83]\n\t"
    "v_and_b32 v123, s76, v115\n\t"
    "v_pk_mul_f32 v[120:121], v[120:121], s[64:65]\n\t"
    "v_pk_mul_f32 v[122:123], v[122:123], s[64:65]\n\t"
    "v_cvt_pk_bf16_f32 v114, v120, v121\n\t"
    "v_cvt_pk_bf16_f32 v115, v122, v123\n\t"
    "s_waitcnt lgkmcnt(0)\n\t"
    "v_lshlrev_b32 v120, 16, v116\n\t"
    "v_and_b32 v121, s76, v116\n\t"
    "v_lshlrev_b32 v122, 16, v117\n\t"
    "v_mfma_f32_16x16x32_bf16 v[32:35], a[148:151], v[112:115], v[32:35]\n\t"
    "v_and_b32 v123, s76, v117\n\t"
    "v_pk_mul_f32 v[120:121], v[120:121], s[64:65]\n\t"
    "v_pk_mul_f32 v[122:123], v[122:123], s[64:65]\n\t"
    "v_cvt_pk_bf16_f32 v116, v120, v121\n\t"
    "v_cvt_pk_bf16_f32 v117, v122, v123\n\t"
    "v_lshlrev_b32 v120, 16, v118\n\t"
    "v_and_b32 v121, s76, v118\n\t"
    "v_lshlrev_b32 v122, 16, v119\n\t"
    "v_mfma_f32_16x16x32_bf16 v[80:83], a[172:175], v[112:115], v[80:83]\n\t"
    "v_and_b32 v123, s76, v119\n\t"
    "v_pk_mul_f32 v[120:121], v[120:121], s[64:65]\n\t"
    "v_pk_mul_f32 v[122:123], v[122:123], s[64:65]\n\t"
    "v_cvt_pk_bf16_f32 v118, v120, v121\n\t"
    "v_cvt_pk_bf16_f32 v119, v122, v123\n\t"
    "s_nop 1\n\t"
    "v_mfma_f32_16x16x32_bf16 v[32:35], a[152:155], v[116:119], v[32:35]\n\t"
    "v_mfma_f32_16x16x32_bf16 v[80:83], a[176:179], v[116:119], v[80:83]\n\t"
    "s_waitcnt vmcnt(6)\n\t"
    "ds_read_b128 v[44:47], v92 offset:0\n\t"
    "ds_read_b128 v[48:51], v93 offset:0\n\t"
    "ds_read_b128 v[52:55], v92 offset:128\n\t"
    "ds_read_b128 v[56:59], v93 offset:128\n\t"
    "ds_read_b128 v[60:63], v92 offset:256\n\t"
    "ds_read_b128 v[64:67], v93 offset:256\n\t"
    "v_add_u32 v92, 0x6000, v92\n\t"
    "v_add_u32 v93, 0x6000, v93\n\t"
    "s_add_u32 s70, s53, 12288\n\t"
    "s_add_u32 m0, s53, 0\n\t"
    "s_nop 0\n\t"
    "buffer_load_dwordx4 v16, s[40:43], s70 offen offset:0 sc1 nt lds\n\t"
    "buffer_load_dwordx4 v17, s[40:43], s70 offen offset:1024 sc1 nt lds\n\t"
    "buffer_load_dwordx4 v18, s[40:43], s70 offen offset:2048 sc1 nt lds\n\t"
    "v_accvgpr_write_b32 a72, v96\n\t"
    "v_accvgpr_write_b32 a73, v97\n\t"
    "v_accvgpr_write_b32 a74, v98\n\t"
    "v_accvgpr_write_b32 a75, v99\n\t"
    "v_accvgpr_write_b32 a76, v100\n\t"
    "v_accvgpr_write_b32 a77, v101\n\t"
    "v_accvgpr_write_b32 a78, v102\n\t"
    "v_accvgpr_write_b32 a79, v103\n\t"
    "v_accvgpr_write_b32 a80, v104\n\t"
    "v_accvgpr_write_b32 a81, v105\n\t"
    "v_accvgpr_write_b32 a82, v106\n\t"
    "v_accvgpr_write_b32 a83, v107\n\t"
    "v_accvgpr_write_b32 a84, v108\n\t"
    "v_accvgpr_write_b32 a85, v109\n\t"
    "v_accvgpr_write_b32 a86, v110\n\t"
    "v_accvgpr_write_b32 a87, v111\n\t"
    "v_accvgpr_write_b32 a88, v112\n\t"
    "v_accvgpr_write_b32 a89, v113\n\t"
    "v_accvgpr_write_b32 a90, v114\n\t"
    "v_accvgpr_write_b32 a91, v115\n\t"
    "v_accvgpr_write_b32 a92, v116\n\t"
    "v_accvgpr_write_b32 a93, v117\n\t"
    "v_accvgpr_write_b32 a94, v118\n\t"
    "v_accvgpr_write_b32 a95, v119\n\t"
    "v_exp_f32 v32, v32\n\t"
    "v_exp_f32 v33, v33\n\t"
    "s_nop 0\n\t"
    "v_add_f32 v7, v7, v32\n\t"
    "v_add_f32 v7, v7, v33\n\t"
    "v_cvt_pk_bf16_f32 v32, v32, v33\n\t"
    "v_exp_f32 v34, v34\n\t"
    "v_exp_f32 v35, v35\n\t"
    "s_nop 0\n\t"
    "v_add_f32 v7, v7, v34\n\t"
    "v_add_f32 v7, v7, v35\n\t"
    "v_cvt_pk_bf16_f32 v33, v34, v35\n\t"
    "s_waitcnt lgkmcnt(5)\n\t"
    "v_lshlrev_b32 v120, 16, v44\n\t"
    "v_and_b32 v121, s76, v44\n\t"
    "v_lshlrev_b32 v122, 16, v45\n\t"
    "v_and_b32 v123, s76, v45\n\t"
    "v_pk_mul_f32 v[120:121], v[120:121], s[64:65]\n\t"
    "v_pk_mul_f32 v[122:123], v[122:123], s[64:65]\n\t"
    "v_cvt_pk_bf16_f32 v44, v120, v121\n\t"
    "v_cvt_pk_bf16_f32 v45, v122, v123\n\t"
    "v_lshlrev_b32 v120, 16, v46\n\t"
    "v_and_b32 v121, s76, v46\n\t"
    "v_lshlrev_b32 v122, 16, v47\n\t"
    "v_and_b32 v123, s76, v47\n\t"
    "v_pk_mul_f32 v[120:121], v[120:121], s[64:65]\n\t"
    "v_pk_mul_f32 v[122:123], v[122:123], s[64:65]\n\t"
    "v_cvt_pk_bf16_f32 v46, v120, v121\n\t"
    "v_cvt_pk_bf16_f32 v47, v122, v123\n\t"
    "s_waitcnt lgkmcnt(4)\n\t"
    "v_lshlrev_b32 v120, 16, v48\n\t"
    "v_and_b32 v121, s76, v48\n\t"
    "s_add_u32 s70, s53, 0\n\t"
    "s_add_u32 m0, s53, 12288\n\t"
    "s_nop 0\n\t"
    "buffer_load_dwordx4 v16, s[44:47], s70 offen offset:0 sc1 nt lds\n\t"
    "buffer_load_dwordx4 v17, s[44:47], s70 offen offset:1024 sc1 nt lds\n\t"
    "buffer_load_dwordx4 v18, s[44:47], s70 offen offset:2048 sc1 nt lds\n\t"
    "v_lshlrev_b32 v122, 16, v49\n\t"
    "v_mfma_f32_16x16x32_bf16 v[36:39], a[132:135], v[44:47], 0\n\t"
    "v_and_b32 v123, s76, v49\n\t"
    "v_pk_mul_f32 v[120:121], v[120:121], s[64:65]\n\t"
    "v_pk_mul_f32 v[122:123], v[122:123], s[64:65]\n\t"
    "v_cvt_pk_bf16_f32 v48, v120, v121\n\t"
    "v_cvt_pk_bf16_f32 v49, v122, v123\n\t"
    "v_lshlrev_b32 v120, 16, v50\n\t"
    "v_and_b32 v121, s76, v50\n\t"
    "v_lshlrev_b32 v122, 16, v51\n\t"
    "v_mfma_f32_16x16x32_bf16 v[84:87], a[156:159], v[44:47], 0\n\t"
    "v_and_b32 v123, s76, v51\n\t"
    "v_pk_mul_f32 v[120:121], v[120:121], s[64:65]\n\t"
    "v_pk_mul_f32 v[122:123], v[122:123], s[64:65]\n\t"
    "v_cvt_pk_bf16_f32 v50, v120, v121\n\t"
    "v_cvt_pk_bf16_f32 v51, v122, v123\n\t"
    "s_waitcnt lgkmcnt(3)\n\t"
    "v_lshlrev_b32 v120, 16, v52\n\t"
    "v_and_b32 v121, s76, v52\n\t"
    "v_lshlrev_b32 v122, 16, v53\n\t"
    "v_mfma_f32_16x16x32_bf16 v[36:39], a[136:139], v[48:51], v[36:39]\n\t"
    "v_and_b32 v123, s76, v53\n\t"
    "v_pk_mul_f32 v[120:121], v[120:121], s[64:65]\n\t"
    "v_pk_mul_f32 v[122:123], v[122:123], s[64:65]\n\t"
    "v_cvt_pk_bf16_f32 v52, v120, v121\n\t"
    "v_cvt_pk_bf16_f32 v53, v122, v123\n\t"
    "v_lshlrev_b32 v120, 16, v54\n\t"
    "v_and_b32 v121, s76, v54\n\t"
    "v_lshlrev_b32 v122, 16, v55\n\t"
    "v_mfma_f32_16x16x32_bf16 v[84:87], a[160:163], v[48:51], v[84:87]\n\t"
    "v_and_b32 v123, s76, v55\n\t"
    "v_pk_mul_f32 v[120:121], v[120:121], s[64:65]\n\t"
    "v_pk_mul_f32 v[122:123], v[122:123], s[64:65]\n\t"
    "v_cvt_pk_bf16_f32 v54, v120, v121\n\t"
    "v_cvt_pk_bf16_f32 v55, v122, v123\n\t"
    "s_waitcnt lgkmcnt(2)\n\t"
    "v_lshlrev_b32 v120, 16, v56\n\t"
    "v_and_b32 v121, s76, v56\n\t"
    "v_lshlrev_b32 v122, 16, v57\n\t"
    "v_mfma_f32_16x16x32_bf16 v[36:39], a[140:143], v[52:55], v[36:39]\n\t"
    "v_and_b32 v123, s76, v57\n\t"
    "v_pk_mul_f32 v[120:121], v[120:121], s[64:65]\n\t"
    "v_pk_mul_f32 v[122:123], v[122:123], s[64:65]\n\t"
    "v_cvt_pk_bf16_f32 v56, v120, v121\n\t"
    "v_cvt_pk_bf16_f32 v57, v122, v123\n\t"
    "v_lshlrev_b32 v120, 16, v58\n\t"
    "v_and_b32 v121, s76, v58\n\t"
    "v_lshlrev_b32 v122, 16, v59\n\t"
    "v_mfma_f32_16x16x32_bf16 v[84:87], a[164:167], v[52:55], v[84:87]\n\t"
    "v_and_b32 v123, s76, v59\n\t"
    "v_pk_mul_f32 v[120:121], v[120:121], s[64:65]\n\t"
    "v_pk_mul_f32 v[122:123], v[122:123], s[64:65]\n\t"
    "v_cvt_pk_bf16_f32 v58, v120, v121\n\t"
    "v_cvt_pk_bf16_f32 v59, v122, v123\n\t"
    "s_waitcnt lgkmcnt(1)\n\t"
    "v_lshlrev_b32 v120, 16, v60\n\t"
    "v_and_b32 v121, s76, v60\n\t"
    "v_lshlrev_b32 v122, 16, v61\n\t"
    "v_mfma_f32_16x16x32_bf16 v[36:39], a[144:147], v[56:59], v[36:39]\n\t"
    "v_and_b32 v123, s76, v61\n\t"
    "v_pk_mul_f32 v[120:121], v[120:121], s[64:65]\n\t"
    "v_pk_mul_f32 v[122:123], v[122:123], s[64:65]\n\t"
    "v_cvt_pk_bf16_f32 v60, v120, v121\n\t"
    "v_cvt_pk_bf16_f32 v61, v122, v123\n\t"
    "v_lshlrev_b32 v120, 16, v62\n\t"
    "v_and_b32 v121, s76, v62\n\t"
    "v_lshlrev_b32 v122, 16, v63\n\t"
    "v_mfma_f32_16x16x32_bf16 v[84:87], a[168:171], v[56:59], v[84:87]\n\t"
    "v_and_b32 v123, s76, v63\n\t"
    "v_pk_mul_f32 v[120:121], v[120:121], s[64:65]\n\t"
    "v_pk_mul_f32 v[122:123], v[122:123], s[64:65]\n\t"
    "v_cvt_pk_bf16_f32 v62, v120, v121\n\t"
    "v_cvt_pk_bf16_f32 v63, v122, v123\n\t"
    "s_waitcnt lgkmcnt(0)\n\t"
    "v_lshlrev_b32 v120, 16, v64\n\t"
    "v_and_b32 v121, s76, v64\n\t"
    "v_lshlrev_b32 v122, 16, v65\n\t"
    "v_mfma_f32_16x16x32_bf16 v[36:39], a[148:151], v[60:63], v[36:39]\n\t"
    "v_and_b32 v123, s76, v65\n\t"
    "v_pk_mul_f32 v[120:121], v[120:121], s[64:65]\n\t"
    "v_pk_mul_f32 v[122:123], v[122:123], s[64:65]\n\t"
    "v_cvt_pk_bf16_f32 v64, v120, v121\n\t"
    "v_cvt_pk_bf16_f32 v65, v122, v123\n\t"
    "v_lshlrev_b32 v120, 16, v66\n\t"
    "v_and_b32 v121, s76, v66\n\t"
    "v_lshlrev_b32 v122, 16, v67\n\t"
    "v_mfma_f32_16x16x32_bf16 v[84:87], a[172:175], v[60:63], v[84:87]\n\t"
    "v_and_b32 v123, s76, v67\n\t"
    "v_pk_mul_f32 v[120:121], v[120:121], s[64:65]\n\t"
    "v_pk_mul_f32 v[122:123], v[122:123], s[64:65]\n\t"
    "v_cvt_pk_bf16_f32 v66, v120, v121\n\t"
    "v_cvt_pk_bf16_f32 v67, v122, v123\n\t"
    "s_nop 1\n\t"
    "v_mfma_f32_16x16x32_bf16 v[36:39], a[152:155], v[64:67], v[36:39]\n\t"
    "v_mfma_f32_16x16x32_bf16 v[84:87], a[176:179], v[64:67], v[84:87]\n\t"
    "s_waitcnt vmcnt(6)\n\t"
    "ds_read_b128 v[96:99], v92 offset:0\n\t"
    "ds_read_b128 v[100:103], v93 offset:0\n\t"
    "ds_read_b128 v[104:107], v92 offset:128\n\t"
    "ds_read_b128 v[108:111], v93 offset:128\n\t"
    "ds_read_b128 v[112:115], v92 offset:256\n\t"
    "ds_read_b128 v[116:119], v93 offset:256\n\t"
    "s_add_u32 s70, s53, 24576\n\t"
    "s_add_u32 m0, s53, 24576\n\t"
    "s_nop 0\n\t"
    "buffer_load_dwordx4 v16, s[40:43], s70 offen offset:0 sc1 nt lds\n\t"
    "buffer_load_dwordx4 v17, s[40:43], s70 offen offset:1024 sc1 nt lds\n\t"
    "buffer_load_dwordx4 v18, s[40:43], s70 offen offset:2048 sc1 nt lds\n\t"
    "v_accvgpr_write_b32 a96, v44\n\t"
    "v_accvgpr_write_b32 a97, v45\n\t"
    "v_accvgpr_write_b32 a98, v46\n\t"
    "v_accvgpr_write_b32 a99, v47\n\t"
    "ds_write_b128 v19, v[48:51] offset:0\n\t"
    "v_accvgpr_write_b32 a100, v52\n\t"
    "v_accvgpr_write_b32 a101, v53\n\t"
    "v_accvgpr_write_b32 a102, v54\n\t"
    "v_accvgpr_write_b32 a103, v55\n\t"
    "v_accvgpr_write_b32 a104, v56\n\t"
    "v_accvgpr_write_b32 a105, v57\n\t"
    "v_accvgpr_write_b32 a106, v58\n\t"
    "v_accvgpr_write_b32 a107, v59\n\t"
    "ds_write_b128 v19, v[60:63] offset:1024\n\t"
    "v_accvgpr_write_b32 a108, v64\n\t"
    "v_accvgpr_write_b32 a109, v65\n\t"
    "v_accvgpr_write_b32 a110, v66\n\t"
    "v_accvgpr_write_b32 a111, v67\n\t"
    "v_exp_f32 v36, v36\n\t"
    "v_exp_f32 v37, v37\n\t"
    "s_nop 0\n\t"
    "v_add_f32 v8, v8, v36\n\t"
    "v_add_f32 v8, v8, v37\n\t"
    "v_cvt_pk_bf16_f32 v36, v36, v37\n\t"
    "v_exp_f32 v38, v38\n\t"
    "v_exp_f32 v39, v39\n\t"
    "s_nop 0\n\t"
    "v_add_f32 v8, v8, v38\n\t"
    "v_add_f32 v8, v8, v39\n\t"
    "v_cvt_pk_bf16_f32 v37, v38, v39\n\t"
    "s_waitcnt lgkmcnt(7)\n\t"
    "v_lshlrev_b32 v120, 16, v96\n\t"
    "v_and_b32 v121, s76, v96\n\t"
    "v_lshlrev_b32 v122, 16, v97\n\t"
    "v_and_b32 v123, s76, v97\n\t"
    "v_pk_mul_f32 v[120:121], v[120:121], s[64:65]\n\t"
    "v_pk_mul_f32 v[122:123], v[122:123], s[64:65]\n\t"
    "v_cvt_pk_bf16_f32 v96, v120, v121\n\t"
    "v_cvt_pk_bf16_f32 v97, v122, v123\n\t"
    "v_lshlrev_b32 v120, 16, v98\n\t"
    "v_and_b32 v121, s76, v98\n\t"
    "v_lshlrev_b32 v122, 16, v99\n\t"
    "v_and_b32 v123, s76, v99\n\t"
    "v_pk_mul_f32 v[120:121], v[120:121], s[64:65]\n\t"
    "v_pk_mul_f32 v[122:123], v[122:123], s[64:65]\n\t"
    "v_cvt_pk_bf16_f32 v98, v120, v121\n\t"
    "v_cvt_pk_bf16_f32 v99, v122, v123\n\t"
    "s_waitcnt lgkmcnt(6)\n\t"
    "v_lshlrev_b32 v120, 16, v100\n\t"
    "v_and_b32 v121, s76, v100\n\t"
    "s_add_u32 s70, s53, 12288\n\t"
    "s_add_u32 m0, s53, 36864\n\t"
    "s_nop 0\n\t"
    "buffer_load_dwordx4 v16, s[44:47], s70 offen offset:0 sc1 nt lds\n\t"
    "buffer_load_dwordx4 v17, s[44:47], s70 offen offset:1024 sc1 nt lds\n\t"
    "buffer_load_dwordx4 v18, s[44:47], s70 offen offset:2048 sc1 nt lds\n\t"
    "v_lshlrev_b32 v122, 16, v101\n\t"
    "v_mfma_f32_16x16x32_bf16 v[40:43], a[132:135], v[96:99], 0\n\t"
    "v_and_b32 v123, s76, v101\n\t"
    "v_pk_mul_f32 v[120:121], v[120:121], s[64:65]\n\t"
    "v_pk_mul_f32 v[122:123], v[122:123], s[64:65]\n\t"
    "v_cvt_pk_bf16_f32 v100, v120, v121\n\t"
    "v_cvt_pk_bf16_f32 v101, v122, v123\n\t"
    "v_lshlrev_b32 v120, 16, v102\n\t"
    "v_and_b32 v121, s76, v102\n\t"
    "v_lshlrev_b32 v122, 16, v103\n\t"
    "v_mfma_f32_16x16x32_bf16 v[88:91], a[156:159], v[96:99], 0\n\t"
    "v_and_b32 v123, s76, v103\n\t"
    "v_pk_mul_f32 v[120:121], v[120:121], s[64:65]\n\t"
    "v_pk_mul_f32 v[122:123], v[122:123], s[64:65]\n\t"
    "v_cvt_pk_bf16_f32 v102, v120, v121\n\t"
    "v_cvt_pk_bf16_f32 v103, v122, v123\n\t"
    "s_waitcnt lgkmcnt(5)\n\t"
    "v_lshlrev_b32 v120, 16, v104\n\t"
    "v_and_b32 v121, s76, v104\n\t"
    "v_lshlrev_b32 v122, 16, v105\n\t"
    "v_mfma_f32_16x16x32_bf16 v[40:43], a[136:139], v[100:103], v[40:43]\n\t"
    "v_and_b32 v123, s76, v105\n\t"
    "v_pk_mul_f32 v[120:121], v[120:121], s[64:65]\n\t"
    "v_pk_mul_f32 v[122:123], v[122:123], s[64:65]\n\t"
    "v_cvt_pk_bf16_f32 v104, v120, v121\n\t"
    "v_cvt_pk_bf16_f32 v105, v122, v123\n\t"
    "v_lshlrev_b32 v120, 16, v106\n\t"
    "v_and_b32 v121, s76, v106\n\t"
    "v_lshlrev_b32 v122, 16, v107\n\t"
    "v_mfma_f32_16x16x32_bf16 v[88:91], a[160:163], v[100:103], v[88:91]\n\t"
    "v_and_b32 v123, s76, v107\n\t"
    "v_pk_mul_f32 v[120:121], v[120:121], s[64:65]\n\t"
    "v_pk_mul_f32 v[122:123], v[122:123], s[64:65]\n\t"
    "v_cvt_pk_bf16_f32 v106, v120, v121\n\t"
    "v_cvt_pk_bf16_f32 v107, v122, v123\n\t"
    "s_waitcnt lgkmcnt(4)\n\t"
    "v_lshlrev_b32 v120, 16, v108\n\t"
    "v_and_b32 v121, s76, v108\n\t"
    "v_lshlrev_b32 v122, 16, v109\n\t"
    "v_mfma_f32_16x16x32_bf16 v[40:43], a[140:143], v[104:107], v[40:43]\n\t"
    "v_and_b32 v123, s76, v109\n\t"
    "v_pk_mul_f32 v[120:121], v[120:121], s[64:65]\n\t"
    "v_pk_mul_f32 v[122:123], v[122:123], s[64:65]\n\t"
    "v_cvt_pk_bf16_f32 v108, v120, v121\n\t"
    "v_cvt_pk_bf16_f32 v109, v122, v123\n\t"
    "v_lshlrev_b32 v120, 16, v110\n\t"
    "v_and_b32 v121, s76, v110\n\t"
    "v_lshlrev_b32 v122, 16, v111\n\t"
    "v_mfma_f32_16x16x32_bf16 v[88:91], a[164:167], v[104:107], v[88:91]\n\t"
    "v_and_b32 v123, s76, v111\n\t"
    "v_pk_mul_f32 v[120:121], v[120:121], s[64:65]\n\t"
    "v_pk_mul_f32 v[122:123], v[122:123], s[64:65]\n\t"
    "v_cvt_pk_bf16_f32 v110, v120, v121\n\t"
    "v_cvt_pk_bf16_f32 v111, v122, v123\n\t"
    "s_waitcnt lgkmcnt(3)\n\t"
    "v_lshlrev_b32 v120, 16, v112\n\t"
    "v_and_b32 v121, s76, v112\n\t"
    "v_lshlrev_b32 v122, 16, v113\n\t"
    "v_mfma_f32_16x16x32_bf16 v[40:43], a[144:147], v[108:111], v[40:43]\n\t"
    "v_and_b32 v123, s76, v113\n\t"
    "v_pk_mul_f32 v[120:121], v[120:121], s[64:65]\n\t"
    "v_pk_mul_f32 v[122:123], v[122:123], s[64:65]\n\t"
    "v_cvt_pk_bf16_f32 v112, v120, v121\n\t"
    "v_cvt_pk_bf16_f32 v113, v122, v123\n\t"
    "v_lshlrev_b32 v120, 16, v114\n\t"
    "v_and_b32 v121, s76, v114\n\t"
    "v_lshlrev_b32 v122, 16, v115\n\t"
    "v_mfma_f32_16x16x32_bf16 v[88:91], a[168:171], v[108:111], v[88:91]\n\t"
    "v_and_b32 v123, s76, v115\n\t"
    "v_pk_mul_f32 v[120:121], v[120:121], s[64:65]\n\t"
    "v_pk_mul_f32 v[122:123], v[122:123], s[64:65]\n\t"
    "v_cvt_pk_bf16_f32 v114, v120, v121\n\t"
    "v_cvt_pk_bf16_f32 v115, v122, v123\n\t"
    "s_waitcnt lgkmcnt(2)\n\t"
    "v_lshlrev_b32 v120, 16, v116\n\t"
    "v_and_b32 v121, s76, v116\n\t"
    "v_lshlrev_b32 v122, 16, v117\n\t"
    "v_mfma_f32_16x16x32_bf16 v[40:43], a[148:151], v[112:115], v[40:43]\n\t"
    "v_and_b32 v123, s76, v117\n\t"
    "v_pk_mul_f32 v[120:121], v[120:121], s[64:65]\n\t"
    "v_pk_mul_f32 v[122:123], v[122:123], s[64:65]\n\t"
    "v_cvt_pk_bf16_f32 v116, v120, v121\n\t"
    "v_cvt_pk_bf16_f32 v117, v122, v123\n\t"
    "v_lshlrev_b32 v120, 16, v118\n\t"
    "v_and_b32 v121, s76, v118\n\t"
    "v_lshlrev_b32 v122, 16, v119\n\t"
    "v_mfma_f32_16x16x32_bf16 v[88:91], a[172:175], v[112:115], v[88:91]\n\t"
    "v_and_b32 v123, s76, v119\n\t"
    "v_pk_mul_f32 v[120:121], v[120:121], s[64:65]\n\t"
    "v_pk_mul_f32 v[122:123], v[122:123], s[64:65]\n\t"
    "v_cvt_pk_bf16_f32 v118, v120, v121\n\t"
    "v_cvt_pk_bf16_f32 v119, v122, v123\n\t"
    "s_nop 1\n\t"
    "v_mfma_f32_16x16x32_bf16 v[40:43], a[152:155], v[116:119], v[40:43]\n\t"
    "v_mfma_f32_16x16x32_bf16 v[88:91], a[176:179], v[116:119], v[88:91]\n\t"
    "ds_write_b128 v19, v[96:99] offset:24576\n\t"
    "ds_write_b128 v19, v[100:103] offset:25600\n\t"
    "ds_write_b128 v19, v[104:107] offset:26624\n\t"
    "ds_write_b128 v19, v[108:111] offset:36864\n\t"
    "ds_write_b128 v19, v[112:115] offset:37888\n\t"
    "ds_write_b128 v19, v[116:119] offset:38912\n\t"
    "v_accvgpr_write_b32 a124, 0\n\t"
    "v_accvgpr_write_b32 a125, 0\n\t"
    "v_accvgpr_write_b32 a126, 0\n\t"
    "v_accvgpr_write_b32 a127, 0\n\t"
    "v_mov_b32 v92, 0\n\t"
    "v_mov_b32 v93, 0\n\t"
    "v_mfma_f32_16x16x32_bf16 a[132:135], a[124:127], a[124:127], 0\n\t"
    "v_mfma_f32_16x16x32_bf16 a[136:139], a[124:127], a[124:127], 0\n\t"
    "v_mfma_f32_16x16x32_bf16 a[140:143], a[124:127], a[124:127], 0\n\t"
    "v_mfma_f32_16x16x32_bf16 a[144:147], a[124:127], a[124:127], 0\n\t"
    "v_mfma_f32_16x16x32_bf16 a[148:151], a[124:127], a[124:127], 0\n\t"
    "v_mfma_f32_16x16x32_bf16 a[152:155], a[124:127], a[124:127], 0\n\t"
    "v_mfma_f32_16x16x32_bf16 a[156:159], a[124:127], a[124:127], 0\n\t"
    "v_mfma_f32_16x16x32_bf16 a[160:163], a[124:127], a[124:127], 0\n\t"
    "v_mfma_f32_16x16x32_bf16 a[164:167], a[124:127], a[124:127], 0\n\t"
    "v_mfma_f32_16x16x32_bf16 a[168:171], a[124:127], a[124:127], 0\n\t"
    "v_mfma_f32_16x16x32_bf16 a[172:175], a[124:127], a[124:127], 0\n\t"
    "v_mfma_f32_16x16x32_bf16 a[176:179], a[124:127], a[124:127], 0\n\t"
    "v_mfma_f32_16x16x32_bf16 v[96:99], a[124:127], a[124:127], 0\n\t"
    "v_mfma_f32_16x16x32_bf16 v[100:103], a[124:127], a[124:127], 0\n\t"
    "v_mfma_f32_16x16x32_bf16 v[104:107], a[124:127], a[124:127], 0\n\t"
    "v_mfma_f32_16x16x32_bf16 v[108:111], a[124:127], a[124:127], 0\n\t"
    "v_mfma_f32_16x16x32_bf16 v[112:115], a[124:127], a[124:127], 0\n\t"
    "v_mfma_f32_16x16x32_bf16 v[116:119], a[124:127], a[124:127], 0\n\t"
    "v_mfma_f32_16x16x32_bf16 v[120:123], a[124:127], a[124:127], 0\n\t"
    "v_exp_f32 v40, v40\n\t"
    "v_exp_f32 v41, v41\n\t"
    "s_nop 0\n\t"
    "v_add_f32 v9, v9, v40\n\t"
    "v_add_f32 v9, v9, v41\n\t"
    "v_cvt_pk_bf16_f32 v40, v40, v41\n\t"
    "v_exp_f32 v42, v42\n\t"
    "v_exp_f32 v43, v43\n\t"
    "s_nop 0\n\t"
    "v_add_f32 v9, v9, v42\n\t"
    "v_add_f32 v9, v9, v43\n\t"
    "v_cvt_pk_bf16_f32 v41, v42, v43\n\t"
    "s_waitcnt vmcnt(9)\n\t"
    "s_waitcnt lgkmcnt(0)\n\t"
    "s_barrier\n\t"
    "ds_read_b128 a[112:115], v10 offset:0\n\t"
    "ds_read_b128 a[124:127], v19 offset:24576\n\t"
    "ds_read_b128 a[116:119], v11 offset:0\n\t"
    "ds_read_b128 a[128:131], v19 offset:0\n\t"
    "s_add_u32 s70, s53, 36864\n\t"
    "s_add_u32 m0, s53, 49152\n\t"
    "s_nop 0\n\t"
    "buffer_load_dwordx4 v16, s[40:43], s70 offen offset:0 sc1 nt lds\n\t"
    "buffer_load_dwordx4 v17, s[40:43], s70 offen offset:1024 sc1 nt lds\n\t"
    "buffer_load_dwordx4 v18, s[40:43], s70 offen offset:2048 sc1 nt lds\n\t"
    "s_add_u32 s70, s53, 24576\n\t"
    "s_add_u32 m0, s53, 61440\n\t"
    "s_nop 0\n\t"
    "buffer_load_dwordx4 v16, s[44:47], s70 offen offset:0 sc1 nt lds\n\t"
    "buffer_load_dwordx4 v17, s[44:47], s70 offen offset:1024 sc1 nt lds\n\t"
    "buffer_load_dwordx4 v18, s[44:47], s70 offen offset:2048 sc1 nt lds\n\t"
    "s_waitcnt lgkmcnt(3)\n\t"
    ".p2align 6\n\t"
    "Lloop_%=:\n\t"
    "v_mfma_f32_16x16x32_bf16 v[44:47], a[112:115], a[0:3], 0\n\t"
    "s_sub_u32 s60, s61, s59\n\t"
    "ds_read_b128 a[120:123], v10 offset:128\n\t"
    "v_mfma_f32_16x16x32_bf16 v[48:51], a[112:115], a[24:27], 0\n\t"
    "s_waitcnt lgkmcnt(3)\n\t"
    "v_exp_f32 v80, v80\n\t"
    "v_mfma_f32_16x16x32_bf16 v[64:67], a[112:115], a[124:127], 0\n\t"
    "s_mov_b32 s59, s61\n\t"
    "ds_read_b128 a[124:127], v19 offset:25600\n\t"
    "v_add_f32 v7, v7, v80\n\t"
    "v_mfma_f32_16x16x32_bf16 v[52:55], a[112:115], a[48:51], 0\n\t"
    "s_add_u32 s61, s59, 24576\n\t"
    "v_mfma_f32_16x16x32_bf16 v[56:59], a[112:115], a[72:75], 0\n\t"
    "s_cmp_eq_u32 s61, 135168\n\t"
    "v_exp_f32 v81, v81\n\t"
    "v_mfma_f32_16x16x32_bf16 v[60:63], a[112:115], a[96:99], 0\n\t"
    "s_waitcnt lgkmcnt(2)\n\t"
    "v_cvt_pk_bf16_f32 v34, v80, v81\n\t"
    "v_add_f32 v7, v7, v81\n\t"
    "v_mfma_f32_16x16x32_bf16 v[60:63], a[116:119], a[128:131], v[60:63]\n\t"
    "s_cselect_b32 s61, 12288, s61\n\t"
    "ds_read_b128 a[112:115], v11 offset:128\n\t"
    "v_mfma_f32_16x16x32_bf16 v[44:47], a[116:119], a[4:7], v[44:47]\n\t"
    "s_mov_b32 s55, s56\n\t"
    "v_exp_f32 v82, v82\n\t"
    "v_mfma_f32_16x16x32_bf16 v[48:51], a[116:119], a[28:31], v[48:51]\n\t"
    "s_mov_b32 s56, s57\n\t"
    "ds_read_b128 a[128:131], v19 offset:26624\n\t"
    "v_add_f32 v7, v7, v82\n\t"
    "v_mfma_f32_16x16x32_bf16 v[52:55], a[116:119], a[52:55], v[52:55]\n\t"
    "s_add_u32 s57, s56, 24576\n\t"
    "v_mfma_f32_16x16x32_bf16 v[56:59], a[116:119], a[76:79], v[56:59]\n\t"
    "s_waitcnt lgkmcnt(2)\n\t"
    "v_exp_f32 v83, v83\n\t"
    "v_mfma_f32_16x16x32_bf16 v[64:67], a[116:119], a[124:127], v[64:67]\n\t"
    "s_waitcnt lgkmcnt(3)\n\t"
    "v_cvt_pk_bf16_f32 v35, v82, v83\n\t"
    "v_add_f32 v7, v7, v83\n\t"
    "v_mfma_f32_16x16x32_bf16 v[44:47], a[120:123], a[8:11], v[44:47]\n\t"
    "s_cmp_eq_u32 s57, 98304\n\t"
    "ds_read_b128 a[116:119], v10 offset:256\n\t"
    "v_mfma_f32_16x16x32_bf16 v[48:51], a[120:123], a[32:35], v[48:51]\n\t"
    "s_cselect_b32 s57, 0, s57\n\t"
    "v_exp_f32 v84, v84\n\t"
    "v_mfma_f32_16x16x32_bf16 v[52:55], a[120:123], a[56:59], v[52:55]\n\t"
    "s_waitcnt lgkmcnt(1)\n\t"
    "ds_read_b128 a[124:127], v19 offset:36864\n\t"
    "v_add_f32 v8, v8, v84\n\t"
    "v_mfma_f32_16x16x32_bf16 v[64:67], a[120:123], a[128:131], v[64:67]\n\t"
    "s_sub_u32 s77, s57, s56\n\t"
    "ds_read_b128 a[128:131], v19 offset:1024\n\t"
    "v_mfma_f32_16x16x32_bf16 v[56:59], a[120:123], a[80:83], v[56:59]\n\t"
    "s_add_u32 s58, s58, 24576\n\t"
    "v_exp_f32 v85, v85\n\t"
    "v_mfma_f32_16x16x32_bf16 v[60:63], a[120:123], a[100:103], v[60:63]\n\t"
    "s_waitcnt lgkmcnt(4)\n\t"
    "v_cvt_pk_bf16_f32 v38, v84, v85\n\t"
    "v_add_f32 v8, v8, v85\n\t"
    "v_mfma_f32_16x16x32_bf16 v[44:47], a[112:115], a[12:15], v[44:47]\n\t"
    "s_cmp_eq_u32 s58, 135168\n\t"
    "ds_read_b128 a[120:123], v11 offset:256\n\t"
    "v_mfma_f32_16x16x32_bf16 v[48:51], a[112:115], a[36:39], v[48:51]\n\t"
    "s_waitcnt lgkmcnt(2)\n\t"
    "v_exp_f32 v86, v86\n\t"
    "v_mfma_f32_16x16x32_bf16 v[64:67], a[112:115], a[124:127], v[64:67]\n\t"
    "s_cselect_b32 s58, 12288, s58\n\t"
    "ds_read_b128 a[124:127], v19 offset:37888\n\t"
    "v_add_f32 v8, v8, v86\n\t"
    "v_mfma_f32_16x16x32_bf16 v[52:55], a[112:115], a[60:63], v[52:55]\n\t"
    "s_add_u32 s62, s62, 12288\n\t"
    "v_mfma_f32_16x16x32_bf16 v[56:59], a[112:115], a[84:87], v[56:59]\n\t"
    "s_add_u32 s63, s63, 12288\n\t"
    "v_exp_f32 v87, v87\n\t"
    "v_mfma_f32_16x16x32_bf16 v[60:63], a[112:115], a[104:107], v[60:63]\n\t"
    "s_waitcnt lgkmcnt(2)\n\t"
    "v_cvt_pk_bf16_f32 v39, v86, v87\n\t"
    "v_add_f32 v8, v8, v87\n\t"
    "v_mfma_f32_16x16x32_bf16 v[60:63], a[116:119], a[128:131], v[60:63]\n\t"
    "s_cmp_le_u32 s54, 11\n\t"
    "ds_read_b128 a[112:115], v10 offset:6144\n\t"
    "v_mfma_f32_16x16x32_bf16 v[44:47], a[116:119], a[16:19], v[44:47]\n\t"
    "s_cselect_b32 s42, 196608, 0\n\t"
    "v_exp_f32 v88, v88\n\t"
    "v_mfma_f32_16x16x32_bf16 v[48:51], a[116:119], a[40:43], v[48:51]\n\t"
    "s_cmp_le_u32 s54, 12\n\t"
    "ds_read_b128 a[128:131], v19 offset:38912\n\t"
    "v_add_f32 v9, v9, v88\n\t"
    "v_mfma_f32_16x16x32_bf16 v[52:55], a[116:119], a[64:67], v[52:55]\n\t"
    "s_cselect_b32 s46, 196608, 0\n\t"
    "v_mfma_f32_16x16x32_bf16 v[56:59], a[116:119], a[88:91], v[56:59]\n\t"
    "s_waitcnt lgkmcnt(2)\n\t"
    "v_exp_f32 v89, v89\n\t"
    "v_mfma_f32_16x16x32_bf16 v[64:67], a[116:119], a[124:127], v[64:67]\n\t"
    "s_waitcnt lgkmcnt(3)\n\t"
    "v_cvt_pk_bf16_f32 v42, v88, v89\n\t"
    "v_add_f32 v9, v9, v89\n\t"
    "v_mfma_f32_16x16x32_bf16 v[44:47], a[120:123], a[20:23], v[44:47]\n\t"
    "ds_read_b128 a[116:119], v11 offset:6144\n\t"
    "v_add_u32 v12, s60, v12\n\t"
    "v_mfma_f32_16x16x32_bf16 v[48:51], a[120:123], a[44:47], v[48:51]\n\t"
    "v_exp_f32 v90, v90\n\t"
    "v_mfma_f32_16x16x32_bf16 v[52:55], a[120:123], a[68:71], v[52:55]\n\t"
    "s_waitcnt lgkmcnt(1)\n\t"
    "ds_read_b128 a[124:127], v19 offset:24576\n\t"
    "v_add_f32 v9, v9, v90\n\t"
    "v_mfma_f32_16x16x32_bf16 v[64:67], a[120:123], a[128:131], v[64:67]\n\t"
    "ds_read_b128 a[128:131], v19 offset:0\n\t"
    "v_mfma_f32_16x16x32_bf16 v[56:59], a[120:123], a[92:95], v[56:59]\n\t"
    "v_exp_f32 v91, v91\n\t"
    "v_mfma_f32_16x16x32_bf16 v[60:63], a[120:123], a[108:111], v[60:63]\n\t"
    "s_waitcnt lgkmcnt(4)\n\t"
    "v_cvt_pk_bf16_f32 v43, v90, v91\n\t"
    "v_add_f32 v9, v9, v91\n\t"
    "v_mfma_f32_16x16x32_bf16 v[68:71], a[112:115], a[0:3], 0\n\t"
    "ds_read_b128 a[120:123], v10 offset:6272\n\t"
    "v_add_u32 v13, s60, v13\n\t"
    "v_mfma_f32_16x16x32_bf16 v[72:75], a[112:115], a[24:27], 0\n\t"
    "s_waitcnt lgkmcnt(2)\n\t"
    "v_exp_f32 v44, v44\n\t"
    "v_mfma_f32_16x16x32_bf16 v[88:91], a[112:115], a[124:127], 0\n\t"
    "ds_read_b128 a[124:127], v19 offset:25600\n\t"
    "v_add_f32 v4, v4, v44\n\t"
    "v_mfma_f32_16x16x32_bf16 v[76:79], a[112:115], a[48:51], 0\n\t"
    "v_mfma_f32_16x16x32_bf16 v[80:83], a[112:115], a[72:75], 0\n\t"
    "v_exp_f32 v45, v45\n\t"
    "v_mfma_f32_16x16x32_bf16 v[84:87], a[112:115], a[96:99], 0\n\t"
    "s_waitcnt lgkmcnt(2)\n\t"
    "v_cvt_pk_bf16_f32 v44, v44, v45\n\t"
    "v_add_f32 v4, v4, v45\n\t"
    "v_mfma_f32_16x16x32_bf16 v[84:87], a[116:119], a[128:131], v[84:87]\n\t"
    "ds_read_b128 a[112:115], v11 offset:6272\n\t"
    "v_add_u32 v14, s60, v14\n\t"
    "v_mfma_f32_16x16x32_bf16 v[68:71], a[116:119], a[4:7], v[68:71]\n\t"
    "v_exp_f32 v46, v46\n\t"
    "v_mfma_f32_16x16x32_bf16 v[72:75], a[116:119], a[28:31], v[72:75]\n\t"
    "ds_read_b128 a[128:131], v19 offset:26624\n\t"
    "v_add_f32 v4, v4, v46\n\t"
    "v_mfma_f32_16x16x32_bf16 v[76:79], a[116:119], a[52:55], v[76:79]\n\t"
    "v_mfma_f32_16x16x32_bf16 v[80:83], a[116:119], a[76:79], v[80:83]\n\t"
    "s_waitcnt lgkmcnt(2)\n\t"
    "v_exp_f32 v47, v47\n\t"
    "v_mfma_f32_16x16x32_bf16 v[88:91], a[116:119], a[124:127], v[88:91]\n\t"
    "s_waitcnt lgkmcnt(3)\n\t"
    "v_cvt_pk_bf16_f32 v45, v46, v47\n\t"
    "v_add_f32 v4, v4, v47\n\t"
    "v_mfma_f32_16x16x32_bf16 v[68:71], a[120:123], a[8:11], v[68:71]\n\t"
    "ds_read_b128 a[116:119], v10 offset:6400\n\t"
    "v_add_u32 v15, s60, v15\n\t"
    "v_mfma_f32_16x16x32_bf16 v[72:75], a[120:123], a[32:35], v[72:75]\n\t"
    "v_exp_f32 v48, v48\n\t"
    "v_mfma_f32_16x16x32_bf16 v[76:79], a[120:123], a[56:59], v[76:79]\n\t"
    "s_waitcnt lgkmcnt(1)\n\t"
    "ds_read_b128 a[124:127], v19 offset:36864\n\t"
    "v_add_f32 v5, v5, v48\n\t"
    "v_mfma_f32_16x16x32_bf16 v[88:91], a[120:123], a[128:131], v[88:91]\n\t"
    "ds_read_b128 a[128:131], v19 offset:1024\n\t"
    "v_mfma_f32_16x16x32_bf16 v[80:83], a[120:123], a[80:83], v[80:83]\n\t"
    "v_exp_f32 v49, v49\n\t"
    "v_mfma_f32_16x16x32_bf16 v[84:87], a[120:123], a[100:103], v[84:87]\n\t"
    "s_waitcnt lgkmcnt(4)\n\t"
    "v_cvt_pk_bf16_f32 v48, v48, v49\n\t"
    "v_add_f32 v5, v5, v49\n\t"
    "v_mfma_f32_16x16x32_bf16 v[68:71], a[112:115], a[12:15], v[68:71]\n\t"
    "ds_read_b128 a[120:123], v11 offset:6400\n\t"
    "v_mfma_f32_16x16x32_bf16 v[72:75], a[112:115], a[36:39], v[72:75]\n\t"
    "s_waitcnt lgkmcnt(2)\n\t"
    "v_exp_f32 v50, v50\n\t"
    "v_mfma_f32_16x16x32_bf16 v[88:91], a[112:115], a[124:127], v[88:91]\n\t"
    "s_cmp_eq_u32 s54, 0\n\t"
    "ds_read_b128 a[124:127], v19 offset:37888\n\t"
    "v_add_f32 v5, v5, v50\n\t"
    "v_mfma_f32_16x16x32_bf16 v[76:79], a[112:115], a[60:63], v[76:79]\n\t"
    "s_cbranch_scc1 Lv0_%=\n\t"
    "Lv0b_%=:\n\t"
    "v_mfma_f32_16x16x32_bf16 v[80:83], a[112:115], a[84:87], v[80:83]\n\t"
    "v_exp_f32 v51, v51\n\t"
    "v_mfma_f32_16x16x32_bf16 v[84:87], a[112:115], a[104:107], v[84:87]\n\t"
    "s_waitcnt lgkmcnt(2)\n\t"
    "v_cvt_pk_bf16_f32 v49, v50, v51\n\t"
    "v_add_f32 v5, v5, v51\n\t"
    "v_mfma_f32_16x16x32_bf16 v[84:87], a[116:119], a[128:131], v[84:87]\n\t"
    "ds_read_b64_tr_b16 a[112:113], v12 offset:0\n\t"
    "v_mfma_f32_16x16x32_bf16 v[68:71], a[116:119], a[16:19], v[68:71]\n\t"
    "v_exp_f32 v52, v52\n\t"
    "v_mfma_f32_16x16x32_bf16 v[72:75], a[116:119], a[40:43], v[72:75]\n\t"
    "ds_read_b64_tr_b16 a[114:115], v12 offset:6144\n\t"
    "v_add_f32 v6, v6, v52\n\t"
    "v_mfma_f32_16x16x32_bf16 v[76:79], a[116:119], a[64:67], v[76:79]\n\t"
    "ds_read_b128 a[128:131], v19 offset:38912\n\t"
    "v_mfma_f32_16x16x32_bf16 v[80:83], a[116:119], a[88:91], v[80:83]\n\t"
    "s_waitcnt lgkmcnt(3)\n\t"
    "v_exp_f32 v53, v53\n\t"
    "v_mfma_f32_16x16x32_bf16 v[88:91], a[116:119], a[124:127], v[88:91]\n\t"
    "s_waitcnt lgkmcnt(4)\n\t"
    "v_cvt_pk_bf16_f32 v52, v52, v53\n\t"
    "v_add_f32 v6, v6, v53\n\t"
    "v_mfma_f32_16x16x32_bf16 v[68:71], a[120:123], a[20:23], v[68:71]\n\t"
    "ds_read_b64_tr_b16 a[116:117], v13 offset:0\n\t"
    "v_mfma_f32_16x16x32_bf16 v[72:75], a[120:123], a[44:47], v[72:75]\n\t"
    "v_exp_f32 v54, v54\n\t"
    "v_mfma_f32_16x16x32_bf16 v[76:79], a[120:123], a[68:71], v[76:79]\n\t"
    "s_waitcnt lgkmcnt(1)\n\t"
    "ds_read_b64_tr_b16 a[118:119], v13 offset:6144\n\t"
    "v_add_f32 v6, v6, v54\n\t"
    "v_mfma_f32_16x16x32_bf16 v[88:91], a[120:123], a[128:131], v[88:91]\n\t"
    "v_mfma_f32_16x16x32_bf16 v[80:83], a[120:123], a[92:95], v[80:83]\n\t"
    "v_exp_f32 v55, v55\n\t"
    "v_mfma_f32_16x16x32_bf16 v[84:87], a[120:123], a[108:111], v[84:87]\n\t"
    "s_waitcnt lgkmcnt(3)\n\t"
    "v_cvt_pk_bf16_f32 v53, v54, v55\n\t"
    "v_add_f32 v6, v6, v55\n\t"
    "v_mfma_f32_16x16x32_bf16 v[92:95], a[112:115], v[20:23], v[92:95]\n\t"
    "ds_read_b64_tr_b16 a[120:121], v14 offset:0\n\t"
    "v_mfma_f32_16x16x32_bf16 v[120:123], a[112:115], v[24:27], v[120:123]\n\t"
    "v_exp_f32 v56, v56\n\t"
    "v_mfma_f32_16x16x32_bf16 v[148:151], a[112:115], v[28:31], v[148:151]\n\t"
    "ds_read_b64_tr_b16 a[122:123], v14 offset:6144\n\t"
    "v_add_f32 v7, v7, v56\n\t"
    "v_mfma_f32_16x16x32_bf16 v[176:179], a[112:115], v[32:35], v[176:179]\n\t"
    "v_mfma_f32_16x16x32_bf16 v[204:207], a[112:115], v[36:39], v[204:207]\n\t"
    "v_exp_f32 v57, v57\n\t"
    "v_mfma_f32_16x16x32_bf16 v[232:235], a[112:115], v[40:43], v[232:235]\n\t"
    "s_waitcnt lgkmcnt(2)\n\t"
    "v_cvt_pk_bf16_f32 v56, v56, v57\n\t"
    "v_add_f32 v7, v7, v57\n\t"
    "v_mfma_f32_16x16x32_bf16 v[96:99], a[116:119], v[20:23], v[96:99]\n\t"
    "ds_read_b64_tr_b16 a[112:113], v15 offset:0\n\t"
    "v_mfma_f32_16x16x32_bf16 v[124:127], a[116:119], v[24:27], v[124:127]\n\t"
    "v_exp_f32 v58, v58\n\t"
    "v_mfma_f32_16x16x32_bf16 v[152:155], a[116:119], v[28:31], v[152:155]\n\t"
    "ds_read_b64_tr_b16 a[114:115], v15 offset:6144\n\t"
    "v_add_f32 v7, v7, v58\n\t"
    "v_mfma_f32_16x16x32_bf16 v[180:183], a[116:119], v[32:35], v[180:183]\n\t"
    "v_mfma_f32_16x16x32_bf16 v[208:211], a[116:119], v[36:39], v[208:211]\n\t"
    "v_exp_f32 v59, v59\n\t"
    "v_mfma_f32_16x16x32_bf16 v[236:239], a[116:119], v[40:43], v[236:239]\n\t"
    "s_waitcnt lgkmcnt(2)\n\t"
    "v_cvt_pk_bf16_f32 v57, v58, v59\n\t"
    "v_add_f32 v7, v7, v59\n\t"
    "v_mfma_f32_16x16x32_bf16 v[100:103], a[120:123], v[20:23], v[100:103]\n\t"
    "ds_read_b64_tr_b16 a[116:117], v12 offset:128\n\t"
    "v_add_u32 v10, s77, v10\n\t"
    "v_mfma_f32_16x16x32_bf16 v[128:131], a[120:123], v[24:27], v[128:131]\n\t"
    "v_exp_f32 v60, v60\n\t"
    "v_mfma_f32_16x16x32_bf16 v[156:159], a[120:123], v[28:31], v[156:159]\n\t"
    "ds_read_b64_tr_b16 a[118:119], v12 offset:6272\n\t"
    "v_add_f32 v8, v8, v60\n\t"
    "v_mfma_f32_16x16x32_bf16 v[184:187], a[120:123], v[32:35], v[184:187]\n\t"
    "v_mfma_f32_16x16x32_bf16 v[212:215], a[120:123], v[36:39], v[212:215]\n\t"
    "v_exp_f32 v61, v61\n\t"
    "v_mfma_f32_16x16x32_bf16 v[240:243], a[120:123], v[40:43], v[240:243]\n\t"
    "s_waitcnt lgkmcnt(2)\n\t"
    "v_cvt_pk_bf16_f32 v60, v60, v61\n\t"
    "v_add_f32 v8, v8, v61\n\t"
    "v_mfma_f32_16x16x32_bf16 v[104:107], a[112:115], v[20:23], v[104:107]\n\t"
    "ds_read_b64_tr_b16 a[120:121], v13 offset:128\n\t"
    "v_add_u32 v11, s77, v11\n\t"
    "v_mfma_f32_16x16x32_bf16 v[132:135], a[112:115], v[24:27], v[132:135]\n\t"
    "v_exp_f32 v62, v62\n\t"
    "v_mfma_f32_16x16x32_bf16 v[160:163], a[112:115], v[28:31], v[160:163]\n\t"
    "ds_read_b64_tr_b16 a[122:123], v13 offset:6272\n\t"
    "v_add_f32 v8, v8, v62\n\t"
    "v_mfma_f32_16x16x32_bf16 v[188:191], a[112:115], v[32:35], v[188:191]\n\t"
    "v_mfma_f32_16x16x32_bf16 v[216:219], a[112:115], v[36:39], v[216:219]\n\t"
    "v_exp_f32 v63, v63\n\t"
    "v_mfma_f32_16x16x32_bf16 v[244:247], a[112:115], v[40:43], v[244:247]\n\t"
    "s_waitcnt lgkmcnt(2)\n\t"
    "v_cvt_pk_bf16_f32 v61, v62, v63\n\t"
    "v_add_f32 v8, v8, v63\n\t"
    "v_mfma_f32_16x16x32_bf16 v[108:111], a[116:119], v[20:23], v[108:111]\n\t"
    "ds_read_b64_tr_b16 a[112:113], v14 offset:128\n\t"
    "v_mfma_f32_16x16x32_bf16 v[136:139], a[116:119], v[24:27], v[136:139]\n\t"
    "v_exp_f32 v64, v64\n\t"
    "v_mfma_f32_16x16x32_bf16 v[164:167], a[116:119], v[28:31], v[164:167]\n\t"
    "ds_read_b64_tr_b16 a[114:115], v14 offset:6272\n\t"
    "v_add_f32 v9, v9, v64\n\t"
    "v_mfma_f32_16x16x32_bf16 v[192:195], a[116:119], v[32:35], v[192:195]\n\t"
    "v_mfma_f32_16x16x32_bf16 v[220:223], a[116:119], v[36:39], v[220:223]\n\t"
    "v_exp_f32 v65, v65\n\t"
    "v_mfma_f32_16x16x32_bf16 v[248:251], a[116:119], v[40:43], v[248:251]\n\t"
    "s_waitcnt lgkmcnt(2)\n\t"
    "v_cvt_pk_bf16_f32 v64, v64, v65\n\t"
    "v_add_f32 v9, v9, v65\n\t"
    "v_mfma_f32_16x16x32_bf16 v[112:115], a[120:123], v[20:23], v[112:115]\n\t"
    "ds_read_b64_tr_b16 a[116:117], v15 offset:128\n\t"
    "v_mfma_f32_16x16x32_bf16 v[140:143], a[120:123], v[24:27], v[140:143]\n\t"
    "s_waitcnt vmcnt(6)\n\t"
    "v_exp_f32 v66, v66\n\t"
    "v_mfma_f32_16x16x32_bf16 v[168:171], a[120:123], v[28:31], v[168:171]\n\t"
    "s_barrier\n\t"
    "ds_read_b64_tr_b16 a[118:119], v15 offset:6272\n\t"
    "v_add_f32 v9, v9, v66\n\t"
    "v_mfma_f32_16x16x32_bf16 v[196:199], a[120:123], v[32:35], v[196:199]\n\t"
    "s_add_u32 m0, s55, s53\n\t"
    "v_mfma_f32_16x16x32_bf16 v[224:227], a[120:123], v[36:39], v[224:227]\n\t"
    "v_exp_f32 v67, v67\n\t"
    "v_mfma_f32_16x16x32_bf16 v[252:255], a[120:123], v[40:43], v[252:255]\n\t"
    "s_waitcnt lgkmcnt(2)\n\t"
    "v_cvt_pk_bf16_f32 v65, v66, v67\n\t"
    "v_add_f32 v9, v9, v67\n\t"
    "v_mfma_f32_16x16x32_bf16 v[116:119], a[112:115], v[20:23], v[116:119]\n\t"
    "ds_read_b64_tr_b16 a[120:121], v12 offset:256\n\t"
    "v_mfma_f32_16x16x32_bf16 v[144:147], a[112:115], v[24:27], v[144:147]\n\t"
    "v_exp_f32 v68, v68\n\t"
    "v_mfma_f32_16x16x32_bf16 v[172:175], a[112:115], v[28:31], v[172:175]\n\t"
    "ds_read_b64_tr_b16 a[122:123], v12 offset:6400\n\t"
    "v_add_f32 v4, v4, v68\n\t"
    "v_mfma_f32_16x16x32_bf16 v[200:203], a[112:115], v[32:35], v[200:203]\n\t"
    "buffer_load_dwordx4 v16, s[40:43], s62 offen offset:0 sc1 nt lds\n\t"
    "v_mfma_f32_16x16x32_bf16 v[228:231], a[112:115], v[36:39], v[228:231]\n\t"
    "v_exp_f32 v69, v69\n\t"
    "v_mfma_f32_16x16x32_bf16 a[232:235], a[112:115], v[40:43], a[232:235]\n\t"
    "s_waitcnt lgkmcnt(2)\n\t"
    "v_cvt_pk_bf16_f32 v46, v68, v69\n\t"
    "v_add_f32 v4, v4, v69\n\t"
    "v_mfma_f32_16x16x32_bf16 a[132:135], a[116:119], v[20:23], a[132:135]\n\t"
    "ds_read_b64_tr_b16 a[112:113], v13 offset:256\n\t"
    "v_mfma_f32_16x16x32_bf16 a[152:155], a[116:119], v[24:27], a[152:155]\n\t"
    "v_exp_f32 v70, v70\n\t"
    "v_mfma_f32_16x16x32_bf16 a[172:175], a[116:119], v[28:31], a[172:175]\n\t"
    "ds_read_b64_tr_b16 a[114:115], v13 offset:6400\n\t"
    "v_add_f32 v4, v4, v70\n\t"
    "v_mfma_f32_16x16x32_bf16 a[192:195], a[116:119], v[32:35], a[192:195]\n\t"
    "buffer_load_dwordx4 v17, s[40:43], s62 offen offset:1024 sc1 nt lds\n\t"
    "v_mfma_f32_16x16x32_bf16 a[212:215], a[116:119], v[36:39], a[212:215]\n\t"
    "v_exp_f32 v71, v71\n\t"
    "v_mfma_f32_16x16x32_bf16 a[236:239], a[116:119], v[40:43], a[236:239]\n\t"
    "s_waitcnt lgkmcnt(2)\n\t"
    "v_cvt_pk_bf16_f32 v47, v70, v71\n\t"
    "v_add_f32 v4, v4, v71\n\t"
    "v_mfma_f32_16x16x32_bf16 a[136:139], a[120:123], v[20:23], a[136:139]\n\t"
    "ds_read_b64_tr_b16 a[116:117], v14 offset:256\n\t"
    "v_mfma_f32_16x16x32_bf16 a[156:159], a[120:123], v[24:27], a[156:159]\n\t"
    "v_exp_f32 v72, v72\n\t"
    "v_mfma_f32_16x16x32_bf16 a[176:179], a[120:123], v[28:31], a[176:179]\n\t"
    "ds_read_b64_tr_b16 a[118:119], v14 offset:6400\n\t"
    "v_add_f32 v5, v5, v72\n\t"
    "v_mfma_f32_16x16x32_bf16 a[196:199], a[120:123], v[32:35], a[196:199]\n\t"
    "buffer_load_dwordx4 v18, s[40:43], s62 offen offset:2048 sc1 nt lds\n\t"
    "v_mfma_f32_16x16x32_bf16 a[216:219], a[120:123], v[36:39], a[216:219]\n\t"
    "s_add_u32 m0, s58, s53\n\t"
    "v_exp_f32 v73, v73\n\t"
    "v_mfma_f32_16x16x32_bf16 a[240:243], a[120:123], v[40:43], a[240:243]\n\t"
    "s_waitcnt lgkmcnt(2)\n\t"
    "v_cvt_pk_bf16_f32 v50, v72, v73\n\t"
    "v_add_f32 v5, v5, v73\n\t"
    "v_mfma_f32_16x16x32_bf16 a[140:143], a[112:115], v[20:23], a[140:143]\n\t"
    "ds_read_b64_tr_b16 a[120:121], v15 offset:256\n\t"
    "v_mfma_f32_16x16x32_bf16 a[160:163], a[112:115], v[24:27], a[160:163]\n\t"
    "v_exp_f32 v74, v74\n\t"
    "v_mfma_f32_16x16x32_bf16 a[180:183], a[112:115], v[28:31], a[180:183]\n\t"
    "ds_read_b64_tr_b16 a[122:123], v15 offset:6400\n\t"
    "v_add_f32 v5, v5, v74\n\t"
    "v_mfma_f32_16x16x32_bf16 a[200:203], a[112:115], v[32:35], a[200:203]\n\t"
    "buffer_load_dwordx4 v16, s[44:47], s63 offen offset:0 sc1 nt lds\n\t"
    "v_mfma_f32_16x16x32_bf16 a[220:223], a[112:115], v[36:39], a[220:223]\n\t"
    "v_exp_f32 v75, v75\n\t"
    "v_mfma_f32_16x16x32_bf16 a[244:247], a[112:115], v[40:43], a[244:247]\n\t"
    "s_waitcnt lgkmcnt(2)\n\t"
    "v_cvt_pk_bf16_f32 v51, v74, v75\n\t"
    "v_add_f32 v5, v5, v75\n\t"
    "v_mfma_f32_16x16x32_bf16 a[144:147], a[116:119], v[20:23], a[144:147]\n\t"
    "s_add_u32 s54, s54, 1\n\t"
    "ds_read_b128 a[112:115], v10 offset:0\n\t"
    "v_mfma_f32_16x16x32_bf16 a[164:167], a[116:119], v[24:27], a[164:167]\n\t"
    "s_cmp_eq_u32 s54, 15\n\t"
    "v_exp_f32 v76, v76\n\t"
    "v_mfma_f32_16x16x32_bf16 a[184:187], a[116:119], v[28:31], a[184:187]\n\t"
    "ds_read_b128 a[124:127], v19 offset:24576\n\t"
    "v_add_f32 v6, v6, v76\n\t"
    "v_mfma_f32_16x16x32_bf16 a[204:207], a[116:119], v[32:35], a[204:207]\n\t"
    "buffer_load_dwordx4 v17, s[44:47], s63 offen offset:1024 sc1 nt lds\n\t"
    "v_mfma_f32_16x16x32_bf16 a[224:227], a[116:119], v[36:39], a[224:227]\n\t"
    "v_exp_f32 v77, v77\n\t"
    "v_mfma_f32_16x16x32_bf16 a[248:251], a[116:119], v[40:43], a[248:251]\n\t"
    "s_waitcnt lgkmcnt(2)\n\t"
    "v_cvt_pk_bf16_f32 v54, v76, v77\n\t"
    "v_add_f32 v6, v6, v77\n\t"
    "v_mfma_f32_16x16x32_bf16 a[148:151], a[120:123], v[20:23], a[148:151]\n\t"
    "ds_read_b128 a[116:119], v11 offset:0\n\t"
    "v_mfma_f32_16x16x32_bf16 a[168:171], a[120:123], v[24:27], a[168:171]\n\t"
    "v_exp_f32 v78, v78\n\t"
    "v_mfma_f32_16x16x32_bf16 a[188:191], a[120:123], v[28:31], a[188:191]\n\t"
    "ds_read_b128 a[128:131], v19 offset:0\n\t"
    "v_add_f32 v6, v6, v78\n\t"
    "v_mfma_f32_16x16x32_bf16 a[208:211], a[120:123], v[32:35], a[208:211]\n\t"
    "buffer_load_dwordx4 v18, s[44:47], s63 offen offset:2048 sc1 nt lds\n\t"
    "v_mfma_f32_16x16x32_bf16 a[228:231], a[120:123], v[36:39], a[228:231]\n\t"
    "v_exp_f32 v79, v79\n\t"
    "v_mfma_f32_16x16x32_bf16 a[252:255], a[120:123], v[40:43], a[252:255]\n\t"
    "s_waitcnt lgkmcnt(3)\n\t"
    "v_cvt_pk_bf16_f32 v55, v78, v79\n\t"
    "v_add_f32 v6, v6, v79\n\t"
    "s_cbranch_scc1 Lfinal_%=\n\t"
    "v_mfma_f32_16x16x32_bf16 v[20:23], a[112:115], a[0:3], 0\n\t"
    "s_sub_u32 s60, s61, s59\n\t"
    "ds_read_b128 a[120:123], v10 offset:128\n\t"
    "v_mfma_f32_16x16x32_bf16 v[24:27], a[112:115], a[24:27], 0\n\t"
    "s_waitcnt lgkmcnt(3)\n\t"
    "v_exp_f32 v80, v80\n\t"
    "v_mfma_f32_16x16x32_bf16 v[40:43], a[112:115], a[124:127], 0\n\t"
    "s_mov_b32 s59, s61\n\t"
    "ds_read_b128 a[124:127], v19 offset:25600\n\t"
    "v_add_f32 v7, v7, v80\n\t"
    "v_mfma_f32_16x16x32_bf16 v[28:31], a[112:115], a[48:51], 0\n\t"
    "s_add_u32 s61, s59, 24576\n\t"
    "v_mfma_f32_16x16x32_bf16 v[32:35], a[112:115], a[72:75], 0\n\t"
    "s_cmp_eq_u32 s61, 135168\n\t"
    "v_exp_f32 v81, v81\n\t"
    "v_mfma_f32_16x16x32_bf16 v[36:39], a[112:115], a[96:99], 0\n\t"
    "s_waitcnt lgkmcnt(2)\n\t"
    "v_cvt_pk_bf16_f32 v58, v80, v81\n\t"
    "v_add_f32 v7, v7, v81\n\t"
    "v_mfma_f32_16x16x32_bf16 v[36:39], a[116:119], a[128:131], v[36:39]\n\t"
    "s_cselect_b32 s61, 12288, s61\n\t"
    "ds_read_b128 a[112:115], v11 offset:128\n\t"
    "v_mfma_f32_16x16x32_bf16 v[20:23], a[116:119], a[4:7], v[20:23]\n\t"
    "s_mov_b32 s55, s56\n\t"
    "v_exp_f32 v82, v82\n\t"
    "v_mfma_f32_16x16x32_bf16 v[24:27], a[116:119], a[28:31], v[24:27]\n\t"
    "s_mov_b32 s56, s57\n\t"
    "ds_read_b128 a[128:131], v19 offset:26624\n\t"
    "v_add_f32 v7, v7, v82\n\t"
    "v_mfma_f32_16x16x32_bf16 v[28:31], a[116:119], a[52:55], v[28:31]\n\t"
    "s_add_u32 s57, s56, 24576\n\t"
    "v_mfma_f32_16x16x32_bf16 v[32:35], a[116:119], a[76:79], v[32:35]\n\t"
    "s_waitcnt lgkmcnt(2)\n\t"
    "v_exp_f32 v83, v83\n\t"
    "v_mfma_f32_16x16x32_bf16 v[40:43], a[116:119], a[124:127], v[40:43]\n\t"
    "s_waitcnt lgkmcnt(3)\n\t"
    "v_cvt_pk_bf16_f32 v59, v82, v83\n\t"
    "v_add_f32 v7, v7, v83\n\t"
    "v_mfma_f32_16x16x32_bf16 v[20:23], a[120:123], a[8:11], v[20:23]\n\t"
    "s_cmp_eq_u32 s57, 98304\n\t"
    "ds_read_b128 a[116:119], v10 offset:256\n\t"
    "v_mfma_f32_16x16x32_bf16 v[24:27], a[120:123], a[32:35], v[24:27]\n\t"
    "s_cselect_b32 s57, 0, s57\n\t"
    "v_exp_f32 v84, v84\n\t"
    "v_mfma_f32_16x16x32_bf16 v[28:31], a[120:123], a[56:59], v[28:31]\n\t"
    "s_waitcnt lgkmcnt(1)\n\t"
    "ds_read_b128 a[124:127], v19 offset:36864\n\t"
    "v_add_f32 v8, v8, v84\n\t"
    "v_mfma_f32_16x16x32_bf16 v[40:43], a[120:123], a[128:131], v[40:43]\n\t"
    "s_sub_u32 s77, s57, s56\n\t"
    "ds_read_b128 a[128:131], v19 offset:1024\n\t"
    "v_mfma_f32_16x16x32_bf16 v[32:35], a[120:123], a[80:83], v[32:35]\n\t"
    "s_add_u32 s58, s58, 24576\n\t"
    "v_exp_f32 v85, v85\n\t"
    "v_mfma_f32_16x16x32_bf16 v[36:39], a[120:123], a[100:103], v[36:39]\n\t"
    "s_waitcnt lgkmcnt(4)\n\t"
    "v_cvt_pk_bf16_f32 v62, v84, v85\n\t"
    "v_add_f32 v8, v8, v85\n\t"
    "v_mfma_f32_16x16x32_bf16 v[20:23], a[112:115], a[12:15], v[20:23]\n\t"
    "s_cmp_eq_u32 s58, 135168\n\t"
    "ds_read_b128 a[120:123], v11 offset:256\n\t"
    "v_mfma_f32_16x16x32_bf16 v[24:27], a[112:115], a[36:39], v[24:27]\n\t"
    "s_waitcnt lgkmcnt(2)\n\t"
    "v_exp_f32 v86, v86\n\t"
    "v_mfma_f32_16x16x32_bf16 v[40:43], a[112:115], a[124:127], v[40:43]\n\t"
    "s_cselect_b32 s58, 12288, s58\n\t"
    "ds_read_b128 a[124:127], v19 offset:37888\n\t"
    "v_add_f32 v8, v8, v86\n\t"
    "v_mfma_f32_16x16x32_bf16 v[28:31], a[112:115], a[60:63], v[28:31]\n\t"
    "s_add_u32 s62, s62, 12288\n\t"
    "v_mfma_f32_16x16x32_bf16 v[32:35], a[112:115], a[84:87], v[32:35]\n\t"
    "s_add_u32 s63, s63, 12288\n\t"
    "v_exp_f32 v87, v87\n\t"
    "v_mfma_f32_16x16x32_bf16 v[36:39], a[112:115], a[104:107], v[36:39]\n\t"
    "s_waitcnt lgkmcnt(2)\n\t"
    "v_cvt_pk_bf16_f32 v63, v86, v87\n\t"
    "v_add_f32 v8, v8, v87\n\t"
    "v_mfma_f32_16x16x32_bf16 v[36:39], a[116:119], a[128:131], v[36:39]\n\t"
    "s_cmp_le_u32 s54, 11\n\t"
    "ds_read_b128 a[112:115], v10 offset:6144\n\t"
    "v_mfma_f32_16x16x32_bf16 v[20:23], a[116:119], a[16:19], v[20:23]\n\t"
    "s_cselect_b32 s42, 196608, 0\n\t"
    "v_exp_f32 v88, v88\n\t"
    "v_mfma_f32_16x16x32_bf16 v[24:27], a[116:119], a[40:43], v[24:27]\n\t"
    "s_cmp_le_u32 s54, 12\n\t"
    "ds_read_b128 a[128:131], v19 offset:38912\n\t"
    "v_add_f32 v9, v9, v88\n\t"
    "v_mfma_f32_16x16x32_bf16 v[28:31], a[116:119], a[64:67], v[28:31]\n\t"
    "s_cselect_b32 s46, 196608, 0\n\t"
    "v_mfma_f32_16x16x32_bf16 v[32:35], a[116:119], a[88:91], v[32:35]\n\t"
    "s_waitcnt lgkmcnt(2)\n\t"
    "v_exp_f32 v89, v89\n\t"
    "v_mfma_f32_16x16x32_bf16 v[40:43], a[116:119], a[124:127], v[40:43]\n\t"
    "s_waitcnt lgkmcnt(3)\n\t"
    "v_cvt_pk_bf16_f32 v66, v88, v89\n\t"
    "v_add_f32 v9, v9, v89\n\t"
    "v_mfma_f32_16x16x32_bf16 v[20:23], a[120:123], a[20:23], v[20:23]\n\t"
    "ds_read_b128 a[116:119], v11 offset:6144\n\t"
    "v_add_u32 v12, s60, v12\n\t"
    "v_mfma_f32_16x16x32_bf16 v[24:27], a[120:123], a[44:47], v[24:27]\n\t"
    "v_exp_f32 v90, v90\n\t"
    "v_mfma_f32_16x16x32_bf16 v[28:31], a[120:123], a[68:71], v[28:31]\n\t"
    "s_waitcnt lgkmcnt(1)\n\t"
    "ds_read_b128 a[124:127], v19 offset:24576\n\t"
    "v_add_f32 v9, v9, v90\n\t"
    "v_mfma_f32_16x16x32_bf16 v[40:43], a[120:123], a[128:131], v[40:43]\n\t"
    "ds_read_b128 a[128:131], v19 offset:0\n\t"
    "v_mfma_f32_16x16x32_bf16 v[32:35], a[120:123], a[92:95], v[32:35]\n\t"
    "v_exp_f32 v91, v91\n\t"
    "v_mfma_f32_16x16x32_bf16 v[36:39], a[120:123], a[108:111], v[36:39]\n\t"
    "s_waitcnt lgkmcnt(4)\n\t"
    "v_cvt_pk_bf16_f32 v67, v90, v91\n\t"
    "v_add_f32 v9, v9, v91\n\t"
    "v_mfma_f32_16x16x32_bf16 v[68:71], a[112:115], a[0:3], 0\n\t"
    "ds_read_b128 a[120:123], v10 offset:6272\n\t"
    "v_add_u32 v13, s60, v13\n\t"
    "v_mfma_f32_16x16x32_bf16 v[72:75], a[112:115], a[24:27], 0\n\t"
    "s_waitcnt lgkmcnt(2)\n\t"
    "v_exp_f32 v20, v20\n\t"
    "v_mfma_f32_16x16x32_bf16 v[88:91], a[112:115], a[124:127], 0\n\t"
    "ds_read_b128 a[124:127], v19 offset:25600\n\t"
    "v_add_f32 v4, v4, v20\n\t"
    "v_mfma_f32_16x16x32_bf16 v[76:79], a[112:115], a[48:51], 0\n\t"
    "v_mfma_f32_16x16x32_bf16 v[80:83], a[112:115], a[72:75], 0\n\t"
    "v_exp_f32 v21, v21\n\t"
    "v_mfma_f32_16x16x32_bf16 v[84:87], a[112:115], a[96:99], 0\n\t"
    "s_waitcnt lgkmcnt(2)\n\t"
    "v_cvt_pk_bf16_f32 v20, v20, v21\n\t"
    "v_add_f32 v4, v4, v21\n\t"
    "v_mfma_f32_16x16x32_bf16 v[84:87], a[116:119], a[128:131], v[84:87]\n\t"
    "ds_read_b128 a[112:115], v11 offset:6272\n\t"
    "v_add_u32 v14, s60, v14\n\t"
    "v_mfma_f32_16x16x32_bf16 v[68:71], a[116:119], a[4:7], v[68:71]\n\t"
    "v_exp_f32 v22, v22\n\t"
    "v_mfma_f32_16x16x32_bf16 v[72:75], a[116:119], a[28:31], v[72:75]\n\t"
    "ds_read_b128 a[128:131], v19 offset:26624\n\t"
    "v_add_f32 v4, v4, v22\n\t"
    "v_mfma_f32_16x16x32_bf16 v[76:79], a[116:119], a[52:55], v[76:79]\n\t"
    "v_mfma_f32_16x16x32_bf16 v[80:83], a[116:119], a[76:79], v[80:83]\n\t"
    "s_waitcnt lgkmcnt(2)\n\t"
    "v_exp_f32 v23, v23\n\t"
    "v_mfma_f32_16x16x32_bf16 v[88:91], a[116:119], a[124:127], v[88:91]\n\t"
    "s_waitcnt lgkmcnt(3)\n\t"
    "v_cvt_pk_bf16_f32 v21, v22, v23\n\t"
    "v_add_f32 v4, v4, v23\n\t"
    "v_mfma_f32_16x16x32_bf16 v[68:71], a[120:123], a[8:11], v[68:71]\n\t"
    "ds_read_b128 a[116:119], v10 offset:6400\n\t"
    "v_add_u32 v15, s60, v15\n\t"
    "v_mfma_f32_16x16x32_bf16 v[72:75], a[120:123], a[32:35], v[72:75]\n\t"
    "v_exp_f32 v24, v24\n\t"
    "v_mfma_f32_16x16x32_bf16 v[76:79], a[120:123], a[56:59], v[76:79]\n\t"
    "s_waitcnt lgkmcnt(1)\n\t"
    "ds_read_b128 a[124:127], v19 offset:36864\n\t"
    "v_add_f32 v5, v5, v24\n\t"
    "v_mfma_f32_16x16x32_bf16 v[88:91], a[120:123], a[128:131], v[88:91]\n\t"
    "ds_read_b128 a[128:131], v19 offset:1024\n\t"
    "v_mfma_f32_16x16x32_bf16 v[80:83], a[120:123], a[80:83], v[80:83]\n\t"
    "v_exp_f32 v25, v25\n\t"
    "v_mfma_f32_16x16x32_bf16 v[84:87], a[120:123], a[100:103], v[84:87]\n\t"
    "s_waitcnt lgkmcnt(4)\n\t"
    "v_cvt_pk_bf16_f32 v24, v24, v25\n\t"
    "v_add_f32 v5, v5, v25\n\t"
    "v_mfma_f32_16x16x32_bf16 v[68:71], a[112:115], a[12:15], v[68:71]\n\t"
    "ds_read_b128 a[120:123], v11 offset:6400\n\t"
    "v_mfma_f32_16x16x32_bf16 v[72:75], a[112:115], a[36:39], v[72:75]\n\t"
    "s_waitcnt lgkmcnt(2)\n\t"
    "v_exp_f32 v26, v26\n\t"
    "v_mfma_f32_16x16x32_bf16 v[88:91], a[112:115], a[124:127], v[88:91]\n\t"
    "ds_read_b128 a[124:127], v19 offset:37888\n\t"
    "v_add_f32 v5, v5, v26\n\t"
    "v_mfma_f32_16x16x32_bf16 v[76:79], a[112:115], a[60:63], v[76:79]\n\t"
    "v_mfma_f32_16x16x32_bf16 v[80:83], a[112:115], a[84:87], v[80:83]\n\t"
    "v_exp_f32 v27, v27\n\t"
    "v_mfma_f32_16x16x32_bf16 v[84:87], a[112:115], a[104:107], v[84:87]\n\t"
    "s_waitcnt lgkmcnt(2)\n\t"
    "v_cvt_pk_bf16_f32 v25, v26, v27\n\t"
    "v_add_f32 v5, v5, v27\n\t"
    "v_mfma_f32_16x16x32_bf16 v[84:87], a[116:119], a[128:131], v[84:87]\n\t"
    "ds_read_b64_tr_b16 a[112:113], v12 offset:0\n\t"
    "v_mfma_f32_16x16x32_bf16 v[68:71], a[116:119], a[16:19], v[68:71]\n\t"
    "v_exp_f32 v28, v28\n\t"
    "v_mfma_f32_16x16x32_bf16 v[72:75], a[116:119], a[40:43], v[72:75]\n\t"
    "ds_read_b64_tr_b16 a[114:115], v12 offset:6144\n\t"
    "v_add_f32 v6, v6, v28\n\t"
    "v_mfma_f32_16x16x32_bf16 v[76:79], a[116:119], a[64:67], v[76:79]\n\t"
    "ds_read_b128 a[128:131], v19 offset:38912\n\t"
    "v_mfma_f32_16x16x32_bf16 v[80:83], a[116:119], a[88:91], v[80:83]\n\t"
    "s_waitcnt lgkmcnt(3)\n\t"
    "v_exp_f32 v29, v29\n\t"
    "v_mfma_f32_16x16x32_bf16 v[88:91], a[116:119], a[124:127], v[88:91]\n\t"
    "s_waitcnt lgkmcnt(4)\n\t"
    "v_cvt_pk_bf16_f32 v28, v28, v29\n\t"
    "v_add_f32 v6, v6, v29\n\t"
    "v_mfma_f32_16x16x32_bf16 v[68:71], a[120:123], a[20:23], v[68:71]\n\t"
    "ds_read_b64_tr_b16 a[116:117], v13 offset:0\n\t"
    "v_mfma_f32_16x16x32_bf16 v[72:75], a[120:123], a[44:47], v[72:75]\n\t"
    "v_exp_f32 v30, v30\n\t"
    "v_mfma_f32_16x16x32_bf16 v[76:79], a[120:123], a[68:71], v[76:79]\n\t"
    "s_waitcnt lgkmcnt(1)\n\t"
    "ds_read_b64_tr_b16 a[118:119], v13 offset:6144\n\t"
    "v_add_f32 v6, v6, v30\n\t"
    "v_mfma_f32_16x16x32_bf16 v[88:91], a[120:123], a[128:131], v[88:91]\n\t"
    "v_mfma_f32_16x16x32_bf16 v[80:83], a[120:123], a[92:95], v[80:83]\n\t"
    "v_exp_f32 v31, v31\n\t"
    "v_mfma_f32_16x16x32_bf16 v[84:87], a[120:123], a[108:111], v[84:87]\n\t"
    "s_waitcnt lgkmcnt(3)\n\t"
    "v_cvt_pk_bf16_f32 v29, v30, v31\n\t"
    "v_add_f32 v6, v6, v31\n\t"
    "v_mfma_f32_16x16x32_bf16 v[92:95], a[112:115], v[44:47], v[92:95]\n\t"
    "ds_read_b64_tr_b16 a[120:121], v14 offset:0\n\t"
    "v_mfma_f32_16x16x32_bf16 v[120:123], a[112:115], v[48:51], v[120:123]\n\t"
    "v_exp_f32 v32, v32\n\t"
    "v_mfma_f32_16x16x32_bf16 v[148:151], a[112:115], v[52:55], v[148:151]\n\t"
    "ds_read_b64_tr_b16 a[122:123], v14 offset:6144\n\t"
    "v_add_f32 v7, v7, v32\n\t"
    "v_mfma_f32_16x16x32_bf16 v[176:179], a[112:115], v[56:59], v[176:179]\n\t"
    "v_mfma_f32_16x16x32_bf16 v[204:207], a[112:115], v[60:63], v[204:207]\n\t"
    "v_exp_f32 v33, v33\n\t"
    "v_mfma_f32_16x16x32_bf16 v[232:235], a[112:115], v[64:67], v[232:235]\n\t"
    "s_waitcnt lgkmcnt(2)\n\t"
    "v_cvt_pk_bf16_f32 v32, v32, v33\n\t"
    "v_add_f32 v7, v7, v33\n\t"
    "v_mfma_f32_16x16x32_bf16 v[96:99], a[116:119], v[44:47], v[96:99]\n\t"
    "ds_read_b64_tr_b16 a[112:113], v15 offset:0\n\t"
    "v_mfma_f32_16x16x32_bf16 v[124:127], a[116:119], v[48:51], v[124:127]\n\t"
    "v_exp_f32 v34, v34\n\t"
    "v_mfma_f32_16x16x32_bf16 v[152:155], a[116:119], v[52:55], v[152:155]\n\t"
    "ds_read_b64_tr_b16 a[114:115], v15 offset:6144\n\t"
    "v_add_f32 v7, v7, v34\n\t"
    "v_mfma_f32_16x16x32_bf16 v[180:183], a[116:119], v[56:59], v[180:183]\n\t"
    "v_mfma_f32_16x16x32_bf16 v[208:211], a[116:119], v[60:63], v[208:211]\n\t"
    "v_exp_f32 v35, v35\n\t"
    "v_mfma_f32_16x16x32_bf16 v[236:239], a[116:119], v[64:67], v[236:239]\n\t"
    "s_waitcnt lgkmcnt(2)\n\t"
    "v_cvt_pk_bf16_f32 v33, v34, v35\n\t"
    "v_add_f32 v7, v7, v35\n\t"
    "v_mfma_f32_16x16x32_bf16 v[100:103], a[120:123], v[44:47], v[100:103]\n\t"
    "ds_read_b64_tr_b16 a[116:117], v12 offset:128\n\t"
    "v_add_u32 v10, s77, v10\n\t"
    "v_mfma_f32_16x16x32_bf16 v[128:131], a[120:123], v[48:51], v[128:131]\n\t"
    "v_exp_f32 v36, v36\n\t"
    "v_mfma_f32_16x16x32_bf16 v[156:159], a[120:123], v[52:55], v[156:159]\n\t"
    "ds_read_b64_tr_b16 a[118:119], v12 offset:6272\n\t"
    "v_add_f32 v8, v8, v36\n\t"
    "v_mfma_f32_16x16x32_bf16 v[184:187], a[120:123], v[56:59], v[184:187]\n\t"
    "v_mfma_f32_16x16x32_bf16 v[212:215], a[120:123], v[60:63], v[212:215]\n\t"
    "v_exp_f32 v37, v37\n\t"
    "v_mfma_f32_16x16x32_bf16 v[240:243], a[120:123], v[64:67], v[240:243]\n\t"
    "s_waitcnt lgkmcnt(2)\n\t"
    "v_cvt_pk_bf16_f32 v36, v36, v37\n\t"
    "v_add_f32 v8, v8, v37\n\t"
    "v_mfma_f32_16x16x32_bf16 v[104:107], a[112:115], v[44:47], v[104:107]\n\t"
    "ds_read_b64_tr_b16 a[120:121], v13 offset:128\n\t"
    "v_add_u32 v11, s77, v11\n\t"
    "v_mfma_f32_16x16x32_bf16 v[132:135], a[112:115], v[48:51], v[132:135]\n\t"
    "v_exp_f32 v38, v38\n\t"
    "v_mfma_f32_16x16x32_bf16 v[160:163], a[112:115], v[52:55], v[160:163]\n\t"
    "ds_read_b64_tr_b16 a[122:123], v13 offset:6272\n\t"
    "v_add_f32 v8, v8, v38\n\t"
    "v_mfma_f32_16x16x32_bf16 v[188:191], a[112:115], v[56:59], v[188:191]\n\t"
    "v_mfma_f32_16x16x32_bf16 v[216:219], a[112:115], v[60:63], v[216:219]\n\t"
    "v_exp_f32 v39, v39\n\t"
    "v_mfma_f32_16x16x32_bf16 v[244:247], a[112:115], v[64:67], v[244:247]\n\t"
    "s_waitcnt lgkmcnt(2)\n\t"
    "v_cvt_pk_bf16_f32 v37, v38, v39\n\t"
    "v_add_f32 v8, v8, v39\n\t"
    "v_mfma_f32_16x16x32_bf16 v[108:111], a[116:119], v[44:47], v[108:111]\n\t"
    "ds_read_b64_tr_b16 a[112:113], v14 offset:128\n\t"
    "v_mfma_f32_16x16x32_bf16 v[136:139], a[116:119], v[48:51], v[136:139]\n\t"
    "v_exp_f32 v40, v40\n\t"
    "v_mfma_f32_16x16x32_bf16 v[164:167], a[116:119], v[52:55], v[164:167]\n\t"
    "ds_read_b64_tr_b16 a[114:115], v14 offset:6272\n\t"
    "v_add_f32 v9, v9, v40\n\t"
    "v_mfma_f32_16x16x32_bf16 v[192:195], a[116:119], v[56:59], v[192:195]\n\t"
    "v_mfma_f32_16x16x32_bf16 v[220:223], a[116:119], v[60:63], v[220:223]\n\t"
    "v_exp_f32 v41, v41\n\t"
    "v_mfma_f32_16x16x32_bf16 v[248:251], a[116:119], v[64:67], v[248:251]\n\t"
    "s_waitcnt lgkmcnt(2)\n\t"
    "v_cvt_pk_bf16_f32 v40, v40, v41\n\t"
    "v_add_f32 v9, v9, v41\n\t"
    "v_mfma_f32_16x16x32_bf16 v[112:115], a[120:123], v[44:47], v[112:115]\n\t"
    "ds_read_b64_tr_b16 a[116:117], v15 offset:128\n\t"
    "v_mfma_f32_16x16x32_bf16 v[140:143], a[120:123], v[48:51], v[140:143]\n\t"
    "s_waitcnt vmcnt(6)\n\t"
    "v_exp_f32 v42, v42\n\t"
    "v_mfma_f32_16x16x32_bf16 v[168:171], a[120:123], v[52:55], v[168:171]\n\t"
    "s_barrier\n\t"
    "ds_read_b64_tr_b16 a[118:119], v15 offset:6272\n\t"
    "v_add_f32 v9, v9, v42\n\t"
    "v_mfma_f32_16x16x32_bf16 v[196:199], a[120:123], v[56:59], v[196:199]\n\t"
    "s_add_u32 m0, s55, s53\n\t"
    "v_mfma_f32_16x16x32_bf16 v[224:227], a[120:123], v[60:63], v[224:227]\n\t"
    "v_exp_f32 v43, v43\n\t"
    "v_mfma_f32_16x16x32_bf16 v[252:255], a[120:123], v[64:67], v[252:255]\n\t"
    "s_waitcnt lgkmcnt(2)\n\t"
    "v_cvt_pk_bf16_f32 v41, v42, v43\n\t"
    "v_add_f32 v9, v9, v43\n\t"
    "v_mfma_f32_16x16x32_bf16 v[116:119], a[112:115], v[44:47], v[116:119]\n\t"
    "ds_read_b64_tr_b16 a[120:121], v12 offset:256\n\t"
    "v_mfma_f32_16x16x32_bf16 v[144:147], a[112:115], v[48:51], v[144:147]\n\t"
    "v_exp_f32 v68, v68\n\t"
    "v_mfma_f32_16x16x32_bf16 v[172:175], a[112:115], v[52:55], v[172:175]\n\t"
    "ds_read_b64_tr_b16 a[122:123], v12 offset:6400\n\t"
    "v_add_f32 v4, v4, v68\n\t"
    "v_mfma_f32_16x16x32_bf16 v[200:203], a[112:115], v[56:59], v[200:203]\n\t"
    "buffer_load_dwordx4 v16, s[40:43], s62 offen offset:0 sc1 nt lds\n\t"
    "v_mfma_f32_16x16x32_bf16 v[228:231], a[112:115], v[60:63], v[228:231]\n\t"
    "v_exp_f32 v69, v69\n\t"
    "v_mfma_f32_16x16x32_bf16 a[232:235], a[112:115], v[64:67], a[232:235]\n\t"
    "s_waitcnt lgkmcnt(2)\n\t"
    "v_cvt_pk_bf16_f32 v22, v68, v69\n\t"
    "v_add_f32 v4, v4, v69\n\t"
    "v_mfma_f32_16x16x32_bf16 a[132:135], a[116:119], v[44:47], a[132:135]\n\t"
    "ds_read_b64_tr_b16 a[112:113], v13 offset:256\n\t"
    "v_mfma_f32_16x16x32_bf16 a[152:155], a[116:119], v[48:51], a[152:155]\n\t"
    "v_exp_f32 v70, v70\n\t"
    "v_mfma_f32_16x16x32_bf16 a[172:175], a[116:119], v[52:55], a[172:175]\n\t"
    "ds_read_b64_tr_b16 a[114:115], v13 offset:6400\n\t"
    "v_add_f32 v4, v4, v70\n\t"
    "v_mfma_f32_16x16x32_bf16 a[192:195], a[116:119], v[56:59], a[192:195]\n\t"
    "buffer_load_dwordx4 v17, s[40:43], s62 offen offset:1024 sc1 nt lds\n\t"
    "v_mfma_f32_16x16x32_bf16 a[212:215], a[116:119], v[60:63], a[212:215]\n\t"
    "v_exp_f32 v71, v71\n\t"
    "v_mfma_f32_16x16x32_bf16 a[236:239], a[116:119], v[64:67], a[236:239]\n\t"
    "s_waitcnt lgkmcnt(2)\n\t"
    "v_cvt_pk_bf16_f32 v23, v70, v71\n\t"
    "v_add_f32 v4, v4, v71\n\t"
    "v_mfma_f32_16x16x32_bf16 a[136:139], a[120:123], v[44:47], a[136:139]\n\t"
    "ds_read_b64_tr_b16 a[116:117], v14 offset:256\n\t"
    "v_mfma_f32_16x16x32_bf16 a[156:159], a[120:123], v[48:51], a[156:159]\n\t"
    "v_exp_f32 v72, v72\n\t"
    "v_mfma_f32_16x16x32_bf16 a[176:179], a[120:123], v[52:55], a[176:179]\n\t"
    "ds_read_b64_tr_b16 a[118:119], v14 offset:6400\n\t"
    "v_add_f32 v5, v5, v72\n\t"
    "v_mfma_f32_16x16x32_bf16 a[196:199], a[120:123], v[56:59], a[196:199]\n\t"
    "buffer_load_dwordx4 v18, s[40:43], s62 offen offset:2048 sc1 nt lds\n\t"
    "v_mfma_f32_16x16x32_bf16 a[216:219], a[120:123], v[60:63], a[216:219]\n\t"
    "s_add_u32 m0, s58, s53\n\t"
    "v_exp_f32 v73, v73\n\t"
    "v_mfma_f32_16x16x32_bf16 a[240:243], a[120:123], v[64:67], a[240:243]\n\t"
    "s_waitcnt lgkmcnt(2)\n\t"
    "v_cvt_pk_bf16_f32 v26, v72, v73\n\t"
    "v_add_f32 v5, v5, v73\n\t"
    "v_mfma_f32_16x16x32_bf16 a[140:143], a[112:115], v[44:47], a[140:143]\n\t"
    "ds_read_b64_tr_b16 a[120:121], v15 offset:256\n\t"
    "v_mfma_f32_16x16x32_bf16 a[160:163], a[112:115], v[48:51], a[160:163]\n\t"
    "v_exp_f32 v74, v74\n\t"
    "v_mfma_f32_16x16x32_bf16 a[180:183], a[112:115], v[52:55], a[180:183]\n\t"
    "ds_read_b64_tr_b16 a[122:123], v15 offset:6400\n\t"
    "v_add_f32 v5, v5, v74\n\t"
    "v_mfma_f32_16x16x32_bf16 a[200:203], a[112:115], v[56:59], a[200:203]\n\t"
    "buffer_load_dwordx4 v16, s[44:47], s63 offen offset:0 sc1 nt lds\n\t"
    "v_mfma_f32_16x16x32_bf16 a[220:223], a[112:115], v[60:63], a[220:223]\n\t"
    "v_exp_f32 v75, v75\n\t"
    "v_mfma_f32_16x16x32_bf16 a[244:247], a[112:115], v[64:67], a[244:247]\n\t"
    "s_waitcnt lgkmcnt(2)\n\t"
    "v_cvt_pk_bf16_f32 v27, v74, v75\n\t"
    "v_add_f32 v5, v5, v75\n\t"
    "v_mfma_f32_16x16x32_bf16 a[144:147], a[116:119], v[44:47], a[144:147]\n\t"
    "s_add_u32 s54, s54, 1\n\t"
    "ds_read_b128 a[112:115], v10 offset:0\n\t"
    "v_mfma_f32_16x16x32_bf16 a[164:167], a[116:119], v[48:51], a[164:167]\n\t"
    "s_cmp_eq_u32 s54, 15\n\t"
    "v_exp_f32 v76, v76\n\t"
    "v_mfma_f32_16x16x32_bf16 a[184:187], a[116:119], v[52:55], a[184:187]\n\t"
    "ds_read_b128 a[124:127], v19 offset:24576\n\t"
    "v_add_f32 v6, v6, v76\n\t"
    "v_mfma_f32_16x16x32_bf16 a[204:207], a[116:119], v[56:59], a[204:207]\n\t"
    "buffer_load_dwordx4 v17, s[44:47], s63 offen offset:1024 sc1 nt lds\n\t"
    "v_mfma_f32_16x16x32_bf16 a[224:227], a[116:119], v[60:63], a[224:227]\n\t"
    "v_exp_f32 v77, v77\n\t"
    "v_mfma_f32_16x16x32_bf16 a[248:251], a[116:119], v[64:67], a[248:251]\n\t"
    "s_waitcnt lgkmcnt(2)\n\t"
    "v_cvt_pk_bf16_f32 v30, v76, v77\n\t"
    "v_add_f32 v6, v6, v77\n\t"
    "v_mfma_f32_16x16x32_bf16 a[148:151], a[120:123], v[44:47], a[148:151]\n\t"
    "ds_read_b128 a[116:119], v11 offset:0\n\t"
    "v_mfma_f32_16x16x32_bf16 a[168:171], a[120:123], v[48:51], a[168:171]\n\t"
    "v_exp_f32 v78, v78\n\t"
    "v_mfma_f32_16x16x32_bf16 a[188:191], a[120:123], v[52:55], a[188:191]\n\t"
    "ds_read_b128 a[128:131], v19 offset:0\n\t"
    "v_add_f32 v6, v6, v78\n\t"
    "v_mfma_f32_16x16x32_bf16 a[208:211], a[120:123], v[56:59], a[208:211]\n\t"
    "buffer_load_dwordx4 v18, s[44:47], s63 offen offset:2048 sc1 nt lds\n\t"
    "v_mfma_f32_16x16x32_bf16 a[228:231], a[120:123], v[60:63], a[228:231]\n\t"
    "v_exp_f32 v79, v79\n\t"
    "v_mfma_f32_16x16x32_bf16 a[252:255], a[120:123], v[64:67], a[252:255]\n\t"
    "s_waitcnt lgkmcnt(3)\n\t"
    "v_cvt_pk_bf16_f32 v31, v78, v79\n\t"
    "v_add_f32 v6, v6, v79\n\t"
    "s_branch Lloop_%=\n\t"
    "Lv0_%=:\n\t"
    "s_waitcnt vmcnt(12)\n\t"
    "s_barrier\n\t"
    "s_branch Lv0b_%=\n\t"
    "Lfinal_%=:\n\t"
    "s_waitcnt vmcnt(0)\n\t"
    "s_waitcnt lgkmcnt(0)\n\t"
    "s_sub_u32 s60, s61, s59\n\t"
    "v_add_u32 v12, s60, v12\n\t"
    "v_add_u32 v13, s60, v13\n\t"
    "v_add_u32 v14, s60, v14\n\t"
    "v_add_u32 v15, s60, v15\n\t"
    "ds_read_b64_tr_b16 a[0:1], v12 offset:0\n\t"
    "ds_read_b64_tr_b16 a[2:3], v12 offset:6144\n\t"
    "ds_read_b64_tr_b16 a[4:5], v13 offset:0\n\t"
    "ds_read_b64_tr_b16 a[6:7], v13 offset:6144\n\t"
    "v_xor_b32 v20, 32, v0\n\t"
    "s_add_u32 s70, s53, 98304\n\t"
    "v_subrev_u32 v21, s70, v19\n\t"
    "s_mul_i32 s71, s52, 36864\n\t"
    "ds_read_b64_tr_b16 a[8:9], v14 offset:0\n\t"
    "ds_read_b64_tr_b16 a[10:11], v14 offset:6144\n\t"
    "ds_read_b64_tr_b16 a[12:13], v15 offset:0\n\t"
    "ds_read_b64_tr_b16 a[14:15], v15 offset:6144\n\t"
    "ds_read_b64_tr_b16 a[16:17], v12 offset:128\n\t"
    "ds_read_b64_tr_b16 a[18:19], v12 offset:6272\n\t"
    "ds_read_b64_tr_b16 a[20:21], v13 offset:128\n\t"
    "ds_read_b64_tr_b16 a[22:23], v13 offset:6272\n\t"
    "ds_read_b64_tr_b16 a[24:25], v14 offset:128\n\t"
    "ds_read_b64_tr_b16 a[26:27], v14 offset:6272\n\t"
    "ds_read_b64_tr_b16 a[28:29], v15 offset:128\n\t"
    "ds_read_b64_tr_b16 a[30:31], v15 offset:6272\n\t"
    "ds_read_b64_tr_b16 a[32:33], v12 offset:256\n\t"
    "ds_read_b64_tr_b16 a[34:35], v12 offset:6400\n\t"
    "ds_read_b64_tr_b16 a[36:37], v13 offset:256\n\t"
    "ds_read_b64_tr_b16 a[38:39], v13 offset:6400\n\t"
    "ds_read_b64_tr_b16 a[40:41], v14 offset:256\n\t"
    "ds_read_b64_tr_b16 a[42:43], v14 offset:6400\n\t"
    "ds_read_b64_tr_b16 a[44:45], v15 offset:256\n\t"
    "ds_read_b64_tr_b16 a[46:47], v15 offset:6400\n\t"
    "s_waitcnt lgkmcnt(15)\n\t"
    "v_mfma_f32_16x16x32_bf16 v[92:95], a[0:3], v[44:47], v[92:95]\n\t"
    "v_mov_b32 v22, v4\n\t"
    "s_waitcnt lgkmcnt(15)\n\t"
    "v_mfma_f32_16x16x32_bf16 v[96:99], a[4:7], v[44:47], v[96:99]\n\t"
    "v_permlane16_swap_b32 v4, v22\n\t"
    "s_waitcnt lgkmcnt(15)\n\t"
    "v_mfma_f32_16x16x32_bf16 v[100:103], a[8:11], v[44:47], v[100:103]\n\t"
    "v_add_f32 v4, v4, v22\n\t"
    "s_waitcnt lgkmcnt(15)\n\t"
    "v_mfma_f32_16x16x32_bf16 v[104:107], a[12:15], v[44:47], v[104:107]\n\t"
    "v_mov_b32 v22, v4\n\t"
    "s_waitcnt lgkmcnt(14)\n\t"
    "v_mfma_f32_16x16x32_bf16 v[108:111], a[16:19], v[44:47], v[108:111]\n\t"
    "v_permlane32_swap_b32 v4, v22\n\t"
    "s_waitcnt lgkmcnt(12)\n\t"
    "v_mfma_f32_16x16x32_bf16 v[112:115], a[20:23], v[44:47], v[112:115]\n\t"
    "v_add_f32 v4, v4, v22\n\t"
    "s_waitcnt lgkmcnt(10)\n\t"
    "v_mfma_f32_16x16x32_bf16 v[116:119], a[24:27], v[44:47], v[116:119]\n\t"
    "v_cmp_nle_f32 vcc, s66, v4\n\t"
    "v_rcp_f32 v24, v4\n\t"
    "s_or_b64 s[68:69], s[68:69], vcc\n\t"
    "s_waitcnt lgkmcnt(8)\n\t"
    "v_mfma_f32_16x16x32_bf16 a[132:135], a[28:31], v[44:47], a[132:135]\n\t"
    "v_cmp_nge_f32 vcc, s67, v4\n\t"
    "s_nop 0\n\t"
    "s_or_b64 s[68:69], s[68:69], vcc\n\t"
    "s_waitcnt lgkmcnt(6)\n\t"
    "v_mfma_f32_16x16x32_bf16 a[136:139], a[32:35], v[44:47], a[136:139]\n\t"
    "s_waitcnt lgkmcnt(4)\n\t"
    "v_mfma_f32_16x16x32_bf16 a[140:143], a[36:39], v[44:47], a[140:143]\n\t"
    "s_waitcnt lgkmcnt(2)\n\t"
    "v_mfma_f32_16x16x32_bf16 a[144:147], a[40:43], v[44:47], a[144:147]\n\t"
    "s_waitcnt lgkmcnt(0)\n\t"
    "v_mfma_f32_16x16x32_bf16 a[148:151], a[44:47], v[44:47], a[148:151]\n\t"
    "v_mfma_f32_16x16x32_bf16 v[120:123], a[0:3], v[48:51], v[120:123]\n\t"
    "v_pk_mul_f32 v[92:93], v[92:93], v[24:25] op_sel_hi:[1,0]\n\t"
    "v_pk_mul_f32 v[94:95], v[94:95], v[24:25] op_sel_hi:[1,0]\n\t"
    "v_cvt_pk_bf16_f32 v92, v92, v93\n\t"
    "v_cvt_pk_bf16_f32 v93, v94, v95\n\t"
    "v_exp_f32 v80, v80\n\t"
    "v_exp_f32 v81, v81\n\t"
    "v_mfma_f32_16x16x32_bf16 v[124:127], a[4:7], v[48:51], v[124:127]\n\t"
    "v_pk_mul_f32 v[96:97], v[96:97], v[24:25] op_sel_hi:[1,0]\n\t"
    "v_pk_mul_f32 v[98:99], v[98:99], v[24:25] op_sel_hi:[1,0]\n\t"
    "v_cvt_pk_bf16_f32 v94, v96, v97\n\t"
    "v_cvt_pk_bf16_f32 v95, v98, v99\n\t"
    "v_add_f32 v7, v7, v80\n\t"
    "v_mfma_f32_16x16x32_bf16 v[128:131], a[8:11], v[48:51], v[128:131]\n\t"
    "ds_write_b128 v0, v[92:95] offset:0\n\t"
    "v_pk_mul_f32 v[100:101], v[100:101], v[24:25] op_sel_hi:[1,0]\n\t"
    "v_pk_mul_f32 v[102:103], v[102:103], v[24:25] op_sel_hi:[1,0]\n\t"
    "v_cvt_pk_bf16_f32 v100, v100, v101\n\t"
    "v_cvt_pk_bf16_f32 v101, v102, v103\n\t"
    "v_cvt_pk_bf16_f32 v58, v80, v81\n\t"
    "v_mfma_f32_16x16x32_bf16 v[132:135], a[12:15], v[48:51], v[132:135]\n\t"
    "v_pk_mul_f32 v[104:105], v[104:105], v[24:25] op_sel_hi:[1,0]\n\t"
    "v_pk_mul_f32 v[106:107], v[106:107], v[24:25] op_sel_hi:[1,0]\n\t"
    "v_cvt_pk_bf16_f32 v102, v104, v105\n\t"
    "v_cvt_pk_bf16_f32 v103, v106, v107\n\t"
    "v_mov_b32 v22, v5\n\t"
    "v_add_f32 v7, v7, v81\n\t"
    "v_mfma_f32_16x16x32_bf16 v[136:139], a[16:19], v[48:51], v[136:139]\n\t"
    "ds_write_b128 v20, v[100:103] offset:0\n\t"
    "v_pk_mul_f32 v[108:109], v[108:109], v[24:25] op_sel_hi:[1,0]\n\t"
    "v_pk_mul_f32 v[110:111], v[110:111], v[24:25] op_sel_hi:[1,0]\n\t"
    "v_cvt_pk_bf16_f32 v108, v108, v109\n\t"
    "v_cvt_pk_bf16_f32 v109, v110, v111\n\t"
    "v_permlane16_swap_b32 v5, v22\n\t"
    "v_exp_f32 v82, v82\n\t"
    "v_exp_f32 v83, v83\n\t"
    "v_mfma_f32_16x16x32_bf16 v[140:143], a[20:23], v[48:51], v[140:143]\n\t"
    "v_pk_mul_f32 v[112:113], v[112:113], v[24:25] op_sel_hi:[1,0]\n\t"
    "v_pk_mul_f32 v[114:115], v[114:115], v[24:25] op_sel_hi:[1,0]\n\t"
    "v_cvt_pk_bf16_f32 v110, v112, v113\n\t"
    "v_cvt_pk_bf16_f32 v111, v114, v115\n\t"
    "v_add_f32 v5, v5, v22\n\t"
    "v_add_f32 v7, v7, v82\n\t"
    "v_mfma_f32_16x16x32_bf16 v[144:147], a[24:27], v[48:51], v[144:147]\n\t"
    "ds_write_b128 v0, v[108:111] offset:64\n\t"
    "v_pk_mul_f32 v[116:117], v[116:117], v[24:25] op_sel_hi:[1,0]\n\t"
    "v_pk_mul_f32 v[118:119], v[118:119], v[24:25] op_sel_hi:[1,0]\n\t"
    "v_cvt_pk_bf16_f32 v116, v116, v117\n\t"
    "v_cvt_pk_bf16_f32 v117, v118, v119\n\t"
    "v_mov_b32 v22, v5\n\t"
    "v_cvt_pk_bf16_f32 v59, v82, v83\n\t"
    "v_mfma_f32_16x16x32_bf16 a[152:155], a[28:31], v[48:51], a[152:155]\n\t"
    "v_accvgpr_read_b32 v40, a132\n\t"
    "v_accvgpr_read_b32 v41, a133\n\t"
    "v_accvgpr_read_b32 v42, a134\n\t"
    "v_accvgpr_read_b32 v43, a135\n\t"
    "v_pk_mul_f32 v[40:41], v[40:41], v[24:25] op_sel_hi:[1,0]\n\t"
    "v_pk_mul_f32 v[42:43], v[42:43], v[24:25] op_sel_hi:[1,0]\n\t"
    "v_cvt_pk_bf16_f32 v118, v40, v41\n\t"
    "v_cvt_pk_bf16_f32 v119, v42, v43\n\t"
    "v_permlane32_swap_b32 v5, v22\n\t"
    "v_add_f32 v7, v7, v83\n\t"
    "v_mfma_f32_16x16x32_bf16 a[156:159], a[32:35], v[48:51], a[156:159]\n\t"
    "ds_write_b128 v20, v[116:119] offset:64\n\t"
    "v_accvgpr_read_b32 v28, a136\n\t"
    "v_accvgpr_read_b32 v29, a137\n\t"
    "v_accvgpr_read_b32 v30, a138\n\t"
    "v_accvgpr_read_b32 v31, a139\n\t"
    "v_pk_mul_f32 v[28:29], v[28:29], v[24:25] op_sel_hi:[1,0]\n\t"
    "v_pk_mul_f32 v[30:31], v[30:31], v[24:25] op_sel_hi:[1,0]\n\t"
    "v_cvt_pk_bf16_f32 v28, v28, v29\n\t"
    "v_cvt_pk_bf16_f32 v29, v30, v31\n\t"
    "v_add_f32 v5, v5, v22\n\t"
    "v_exp_f32 v84, v84\n\t"
    "v_exp_f32 v85, v85\n\t"
    "v_mfma_f32_16x16x32_bf16 a[160:163], a[36:39], v[48:51], a[160:163]\n\t"
    "v_accvgpr_read_b32 v32, a140\n\t"
    "v_accvgpr_read_b32 v33, a141\n\t"
    "v_accvgpr_read_b32 v34, a142\n\t"
    "v_accvgpr_read_b32 v35, a143\n\t"
    "v_pk_mul_f32 v[32:33], v[32:33], v[24:25] op_sel_hi:[1,0]\n\t"
    "v_pk_mul_f32 v[34:35], v[34:35], v[24:25] op_sel_hi:[1,0]\n\t"
    "v_cvt_pk_bf16_f32 v30, v32, v33\n\t"
    "v_cvt_pk_bf16_f32 v31, v34, v35\n\t"
    "v_cmp_nle_f32 vcc, s66, v5\n\t"
    "v_rcp_f32 v26, v5\n\t"
    "s_or_b64 s[68:69], s[68:69], vcc\n\t"
    "v_add_f32 v8, v8, v84\n\t"
    "v_mfma_f32_16x16x32_bf16 a[164:167], a[40:43], v[48:51], a[164:167]\n\t"
    "ds_write_b128 v0, v[28:31] offset:128\n\t"
    "v_accvgpr_read_b32 v36, a144\n\t"
    "v_accvgpr_read_b32 v37, a145\n\t"
    "v_accvgpr_read_b32 v38, a146\n\t"
    "v_accvgpr_read_b32 v39, a147\n\t"
    "v_pk_mul_f32 v[36:37], v[36:37], v[24:25] op_sel_hi:[1,0]\n\t"
    "v_pk_mul_f32 v[38:39], v[38:39], v[24:25] op_sel_hi:[1,0]\n\t"
    "v_cvt_pk_bf16_f32 v36, v36, v37\n\t"
    "v_cvt_pk_bf16_f32 v37, v38, v39\n\t"
    "v_cmp_nge_f32 vcc, s67, v5\n\t"
    "s_nop 0\n\t"
    "s_or_b64 s[68:69], s[68:69], vcc\n\t"
    "v_cvt_pk_bf16_f32 v62, v84, v85\n\t"
    "v_mfma_f32_16x16x32_bf16 a[168:171], a[44:47], v[48:51], a[168:171]\n\t"
    "v_accvgpr_read_b32 v40, a148\n\t"
    "v_accvgpr_read_b32 v41, a149\n\t"
    "v_accvgpr_read_b32 v42, a150\n\t"
    "v_accvgpr_read_b32 v43, a151\n\t"
    "v_pk_mul_f32 v[40:41], v[40:41], v[24:25] op_sel_hi:[1,0]\n\t"
    "v_pk_mul_f32 v[42:43], v[42:43], v[24:25] op_sel_hi:[1,0]\n\t"
    "v_cvt_pk_bf16_f32 v38, v40, v41\n\t"
    "v_cvt_pk_bf16_f32 v39, v42, v43\n\t"
    "v_add_f32 v8, v8, v85\n\t"
    "v_mfma_f32_16x16x32_bf16 v[148:151], a[0:3], v[52:55], v[148:151]\n\t"
    "ds_write_b128 v20, v[36:39] offset:128\n\t"
    "ds_read2st64_b64 a[48:51], v1 offset0:0 offset1:3\n\t"
    "ds_read2st64_b64 a[52:55], v2 offset0:1 offset1:4\n\t"
    "ds_read2st64_b64 a[56:59], v3 offset0:2 offset1:5\n\t"
    "ds_read2st64_b64 a[60:63], v1 offset0:24 offset1:27\n\t"
    "ds_read2st64_b64 a[64:67], v2 offset0:25 offset1:28\n\t"
    "ds_read2st64_b64 a[68:71], v3 offset0:26 offset1:29\n\t"
    "v_pk_mul_f32 v[120:121], v[120:121], v[26:27] op_sel_hi:[1,0]\n\t"
    "v_pk_mul_f32 v[122:123], v[122:123], v[26:27] op_sel_hi:[1,0]\n\t"
    "v_cvt_pk_bf16_f32 v120, v120, v121\n\t"
    "v_cvt_pk_bf16_f32 v121, v122, v123\n\t"
    "v_exp_f32 v86, v86\n\t"
    "v_exp_f32 v87, v87\n\t"
    "v_mfma_f32_16x16x32_bf16 v[152:155], a[4:7], v[52:55], v[152:155]\n\t"
    "v_pk_mul_f32 v[124:125], v[124:125], v[26:27] op_sel_hi:[1,0]\n\t"
    "v_pk_mul_f32 v[126:127], v[126:127], v[26:27] op_sel_hi:[1,0]\n\t"
    "v_cvt_pk_bf16_f32 v122, v124, v125\n\t"
    "v_cvt_pk_bf16_f32 v123, v126, v127\n\t"
    "v_mov_b32 v22, v6\n\t"
    "v_add_f32 v8, v8, v86\n\t"
    "v_mfma_f32_16x16x32_bf16 v[156:159], a[8:11], v[52:55], v[156:159]\n\t"
    "ds_write_b128 v0, v[120:123] offset:24576\n\t"
    "v_pk_mul_f32 v[128:129], v[128:129], v[26:27] op_sel_hi:[1,0]\n\t"
    "v_pk_mul_f32 v[130:131], v[130:131], v[26:27] op_sel_hi:[1,0]\n\t"
    "v_cvt_pk_bf16_f32 v128, v128, v129\n\t"
    "v_cvt_pk_bf16_f32 v129, v130, v131\n\t"
    "v_permlane16_swap_b32 v6, v22\n\t"
    "v_cvt_pk_bf16_f32 v63, v86, v87\n\t"
    "v_mfma_f32_16x16x32_bf16 v[160:163], a[12:15], v[52:55], v[160:163]\n\t"
    "v_pk_mul_f32 v[132:133], v[132:133], v[26:27] op_sel_hi:[1,0]\n\t"
    "v_pk_mul_f32 v[134:135], v[134:135], v[26:27] op_sel_hi:[1,0]\n\t"
    "v_cvt_pk_bf16_f32 v130, v132, v133\n\t"
    "v_cvt_pk_bf16_f32 v131, v134, v135\n\t"
    "s_add_u32 s70, s71, 0\n\t"
    "s_waitcnt lgkmcnt(6)\n\t"
    "buffer_store_dwordx4 a[48:51], v21, s[48:51], s70 offen offset:0 sc0 sc1\n\t"
    "v_add_f32 v6, v6, v22\n\t"
    "v_add_f32 v8, v8, v87\n\t"
    "v_mfma_f32_16x16x32_bf16 v[164:167], a[16:19], v[52:55], v[164:167]\n\t"
    "ds_write_b128 v20, v[128:131] offset:24576\n\t"
    "v_pk_mul_f32 v[136:137], v[136:137], v[26:27] op_sel_hi:[1,0]\n\t"
    "v_pk_mul_f32 v[138:139], v[138:139], v[26:27] op_sel_hi:[1,0]\n\t"
    "v_cvt_pk_bf16_f32 v136, v136, v137\n\t"
    "v_cvt_pk_bf16_f32 v137, v138, v139\n\t"
    "s_waitcnt lgkmcnt(6)\n\t"
    "buffer_store_dwordx4 a[52:55], v21, s[48:51], s70 offen offset:1024 sc0 sc1\n\t"
    "v_mov_b32 v22, v6\n\t"
    "v_exp_f32 v88, v88\n\t"
    "v_exp_f32 v89, v89\n\t"
    "v_mfma_f32_16x16x32_bf16 v[168:171], a[20:23], v[52:55], v[168:171]\n\t"
    "v_pk_mul_f32 v[140:141], v[140:141], v[26:27] op_sel_hi:[1,0]\n\t"
    "v_pk_mul_f32 v[142:143], v[142:143], v[26:27] op_sel_hi:[1,0]\n\t"
    "v_cvt_pk_bf16_f32 v138, v140, v141\n\t"
    "v_cvt_pk_bf16_f32 v139, v142, v143\n\t"
    "s_waitcnt lgkmcnt(5)\n\t"
    "buffer_store_dwordx4 a[56:59], v21, s[48:51], s70 offen offset:2048 sc0 sc1\n\t"
    "v_permlane32_swap_b32 v6, v22\n\t"
    "v_add_f32 v9, v9, v88\n\t"
    "v_mfma_f32_16x16x32_bf16 v[172:175], a[24:27], v[52:55], v[172:175]\n\t"
    "ds_write_b128 v0, v[136:139] offset:24640\n\t"
    "v_pk_mul_f32 v[144:145], v[144:145], v[26:27] op_sel_hi:[1,0]\n\t"
    "v_pk_mul_f32 v[146:147], v[146:147], v[26:27] op_sel_hi:[1,0]\n\t"
    "v_cvt_pk_bf16_f32 v144, v144, v145\n\t"
    "v_cvt_pk_bf16_f32 v145, v146, v147\n\t"
    "s_add_u32 s70, s71, 3072\n\t"
    "s_waitcnt lgkmcnt(5)\n\t"
    "buffer_store_dwordx4 a[60:63], v21, s[48:51], s70 offen offset:0 sc0 sc1\n\t"
    "v_add_f32 v6, v6, v22\n\t"
    "v_cvt_pk_bf16_f32 v66, v88, v89\n\t"
    "v_mfma_f32_16x16x32_bf16 a[172:175], a[28:31], v[52:55], a[172:175]\n\t"
    "v_accvgpr_read_b32 v40, a152\n\t"
    "v_accvgpr_read_b32 v41, a153\n\t"
    "v_accvgpr_read_b32 v42, a154\n\t"
    "v_accvgpr_read_b32 v43, a155\n\t"
    "v_pk_mul_f32 v[40:41], v[40:41], v[26:27] op_sel_hi:[1,0]\n\t"
    "v_pk_mul_f32 v[42:43], v[42:43], v[26:27] op_sel_hi:[1,0]\n\t"
    "v_cvt_pk_bf16_f32 v146, v40, v41\n\t"
    "v_cvt_pk_bf16_f32 v147, v42, v43\n\t"
    "s_waitcnt lgkmcnt(4)\n\t"
    "buffer_store_dwordx4 a[64:67], v21, s[48:51], s70 offen offset:1024 sc0 sc1\n\t"
    "v_cmp_nle_f32 vcc, s66, v6\n\t"
    "v_rcp_f32 v24, v6\n\t"
    "s_or_b64 s[68:69], s[68:69], vcc\n\t"
    "v_add_f32 v9, v9, v89\n\t"
    "v_mfma_f32_16x16x32_bf16 a[176:179], a[32:35], v[52:55], a[176:179]\n\t"
    "ds_write_b128 v20, v[144:147] offset:24640\n\t"
    "v_accvgpr_read_b32 v28, a156\n\t"
    "v_accvgpr_read_b32 v29, a157\n\t"
    "v_accvgpr_read_b32 v30, a158\n\t"
    "v_accvgpr_read_b32 v31, a159\n\t"
    "v_pk_mul_f32 v[28:29], v[28:29], v[26:27] op_sel_hi:[1,0]\n\t"
    "v_pk_mul_f32 v[30:31], v[30:31], v[26:27] op_sel_hi:[1,0]\n\t"
    "v_cvt_pk_bf16_f32 v28, v28, v29\n\t"
    "v_cvt_pk_bf16_f32 v29, v30, v31\n\t"
    "s_waitcnt lgkmcnt(4)\n\t"
    "buffer_store_dwordx4 a[68:71], v21, s[48:51], s70 offen offset:2048 sc0 sc1\n\t"
    "v_cmp_nge_f32 vcc, s67, v6\n\t"
    "s_nop 0\n\t"
    "s_or_b64 s[68:69], s[68:69], vcc\n\t"
    "v_exp_f32 v90, v90\n\t"
    "v_exp_f32 v91, v91\n\t"
    "v_mfma_f32_16x16x32_bf16 a[180:183], a[36:39], v[52:55], a[180:183]\n\t"
    "v_accvgpr_read_b32 v32, a160\n\t"
    "v_accvgpr_read_b32 v33, a161\n\t"
    "v_accvgpr_read_b32 v34, a162\n\t"
    "v_accvgpr_read_b32 v35, a163\n\t"
    "v_pk_mul_f32 v[32:33], v[32:33], v[26:27] op_sel_hi:[1,0]\n\t"
    "v_pk_mul_f32 v[34:35], v[34:35], v[26:27] op_sel_hi:[1,0]\n\t"
    "v_cvt_pk_bf16_f32 v30, v32, v33\n\t"
    "v_cvt_pk_bf16_f32 v31, v34, v35\n\t"
    "v_add_f32 v9, v9, v90\n\t"
    "v_mfma_f32_16x16x32_bf16 a[184:187], a[40:43], v[52:55], a[184:187]\n\t"
    "ds_write_b128 v0, v[28:31] offset:24704\n\t"
    "v_accvgpr_read_b32 v36, a164\n\t"
    "v_accvgpr_read_b32 v37, a165\n\t"
    "v_accvgpr_read_b32 v38, a166\n\t"
    "v_accvgpr_read_b32 v39, a167\n\t"
    "v_pk_mul_f32 v[36:37], v[36:37], v[26:27] op_sel_hi:[1,0]\n\t"
    "v_pk_mul_f32 v[38:39], v[38:39], v[26:27] op_sel_hi:[1,0]\n\t"
    "v_cvt_pk_bf16_f32 v36, v36, v37\n\t"
    "v_cvt_pk_bf16_f32 v37, v38, v39\n\t"
    "v_cvt_pk_bf16_f32 v67, v90, v91\n\t"
    "v_mfma_f32_16x16x32_bf16 a[188:191], a[44:47], v[52:55], a[188:191]\n\t"
    "v_accvgpr_read_b32 v40, a168\n\t"
    "v_accvgpr_read_b32 v41, a169\n\t"
    "v_accvgpr_read_b32 v42, a170\n\t"
    "v_accvgpr_read_b32 v43, a171\n\t"
    "v_pk_mul_f32 v[40:41], v[40:41], v[26:27] op_sel_hi:[1,0]\n\t"
    "v_pk_mul_f32 v[42:43], v[42:43], v[26:27] op_sel_hi:[1,0]\n\t"
    "v_cvt_pk_bf16_f32 v38, v40, v41\n\t"
    "v_cvt_pk_bf16_f32 v39, v42, v43\n\t"
    "v_add_f32 v9, v9, v91\n\t"
    "v_mfma_f32_16x16x32_bf16 v[176:179], a[0:3], v[56:59], v[176:179]\n\t"
    "ds_write_b128 v20, v[36:39] offset:24704\n\t"
    "ds_read2st64_b64 a[48:51], v1 offset0:48 offset1:51\n\t"
    "ds_read2st64_b64 a[52:55], v2 offset0:49 offset1:52\n\t"
    "ds_read2st64_b64 a[56:59], v3 offset0:50 offset1:53\n\t"
    "ds_read2st64_b64 a[60:63], v1 offset0:72 offset1:75\n\t"
    "ds_read2st64_b64 a[64:67], v2 offset0:73 offset1:76\n\t"
    "ds_read2st64_b64 a[68:71], v3 offset0:74 offset1:77\n\t"
    "v_pk_mul_f32 v[148:149], v[148:149], v[24:25] op_sel_hi:[1,0]\n\t"
    "v_pk_mul_f32 v[150:151], v[150:151], v[24:25] op_sel_hi:[1,0]\n\t"
    "v_cvt_pk_bf16_f32 v148, v148, v149\n\t"
    "v_cvt_pk_bf16_f32 v149, v150, v151\n\t"
    "v_mfma_f32_16x16x32_bf16 v[180:183], a[4:7], v[56:59], v[180:183]\n\t"
    "v_pk_mul_f32 v[152:153], v[152:153], v[24:25] op_sel_hi:[1,0]\n\t"
    "v_pk_mul_f32 v[154:155], v[154:155], v[24:25] op_sel_hi:[1,0]\n\t"
    "v_cvt_pk_bf16_f32 v150, v152, v153\n\t"
    "v_cvt_pk_bf16_f32 v151, v154, v155\n\t"
    "v_mov_b32 v22, v7\n\t"
    "v_mfma_f32_16x16x32_bf16 v[184:187], a[8:11], v[56:59], v[184:187]\n\t"
    "ds_write_b128 v0, v[148:151] offset:49152\n\t"
    "v_pk_mul_f32 v[156:157], v[156:157], v[24:25] op_sel_hi:[1,0]\n\t"
    "v_pk_mul_f32 v[158:159], v[158:159], v[24:25] op_sel_hi:[1,0]\n\t"
    "v_cvt_pk_bf16_f32 v156, v156, v157\n\t"
    "v_cvt_pk_bf16_f32 v157, v158, v159\n\t"
    "v_permlane16_swap_b32 v7, v22\n\t"
    "v_mfma_f32_16x16x32_bf16 v[188:191], a[12:15], v[56:59], v[188:191]\n\t"
    "v_pk_mul_f32 v[160:161], v[160:161], v[24:25] op_sel_hi:[1,0]\n\t"
    "v_pk_mul_f32 v[162:163], v[162:163], v[24:25] op_sel_hi:[1,0]\n\t"
    "v_cvt_pk_bf16_f32 v158, v160, v161\n\t"
    "v_cvt_pk_bf16_f32 v159, v162, v163\n\t"
    "s_add_u32 s70, s71, 6144\n\t"
    "s_waitcnt lgkmcnt(6)\n\t"
    "buffer_store_dwordx4 a[48:51], v21, s[48:51], s70 offen offset:0 sc0 sc1\n\t"
    "v_add_f32 v7, v7, v22\n\t"
    "v_mfma_f32_16x16x32_bf16 v[192:195], a[16:19], v[56:59], v[192:195]\n\t"
    "ds_write_b128 v20, v[156:159] offset:49152\n\t"
    "v_pk_mul_f32 v[164:165], v[164:165], v[24:25] op_sel_hi:[1,0]\n\t"
    "v_pk_mul_f32 v[166:167], v[166:167], v[24:25] op_sel_hi:[1,0]\n\t"
    "v_cvt_pk_bf16_f32 v164, v164, v165\n\t"
    "v_cvt_pk_bf16_f32 v165, v166, v167\n\t"
    "s_waitcnt lgkmcnt(6)\n\t"
    "buffer_store_dwordx4 a[52:55], v21, s[48:51], s70 offen offset:1024 sc0 sc1\n\t"
    "v_mov_b32 v22, v7\n\t"
    "v_mfma_f32_16x16x32_bf16 v[196:199], a[20:23], v[56:59], v[196:199]\n\t"
    "v_pk_mul_f32 v[168:169], v[168:169], v[24:25] op_sel_hi:[1,0]\n\t"
    "v_pk_mul_f32 v[170:171], v[170:171], v[24:25] op_sel_hi:[1,0]\n\t"
    "v_cvt_pk_bf16_f32 v166, v168, v169\n\t"
    "v_cvt_pk_bf16_f32 v167, v170, v171\n\t"
    "s_waitcnt lgkmcnt(5)\n\t"
    "buffer_store_dwordx4 a[56:59], v21, s[48:51], s70 offen offset:2048 sc0 sc1\n\t"
    "v_permlane32_swap_b32 v7, v22\n\t"
    "v_mfma_f32_16x16x32_bf16 v[200:203], a[24:27], v[56:59], v[200:203]\n\t"
    "ds_write_b128 v0, v[164:167] offset:49216\n\t"
    "v_pk_mul_f32 v[172:173], v[172:173], v[24:25] op_sel_hi:[1,0]\n\t"
    "v_pk_mul_f32 v[174:175], v[174:175], v[24:25] op_sel_hi:[1,0]\n\t"
    "v_cvt_pk_bf16_f32 v172, v172, v173\n\t"
    "v_cvt_pk_bf16_f32 v173, v174, v175\n\t"
    "s_add_u32 s70, s71, 9216\n\t"
    "s_waitcnt lgkmcnt(5)\n\t"
    "buffer_store_dwordx4 a[60:63], v21, s[48:51], s70 offen offset:0 sc0 sc1\n\t"
    "v_add_f32 v7, v7, v22\n\t"
    "v_mfma_f32_16x16x32_bf16 a[192:195], a[28:31], v[56:59], a[192:195]\n\t"
    "v_accvgpr_read_b32 v40, a172\n\t"
    "v_accvgpr_read_b32 v41, a173\n\t"
    "v_accvgpr_read_b32 v42, a174\n\t"
    "v_accvgpr_read_b32 v43, a175\n\t"
    "v_pk_mul_f32 v[40:41], v[40:41], v[24:25] op_sel_hi:[1,0]\n\t"
    "v_pk_mul_f32 v[42:43], v[42:43], v[24:25] op_sel_hi:[1,0]\n\t"
    "v_cvt_pk_bf16_f32 v174, v40, v41\n\t"
    "v_cvt_pk_bf16_f32 v175, v42, v43\n\t"
    "s_waitcnt lgkmcnt(4)\n\t"
    "buffer_store_dwordx4 a[64:67], v21, s[48:51], s70 offen offset:1024 sc0 sc1\n\t"
    "v_cmp_nle_f32 vcc, s66, v7\n\t"
    "v_rcp_f32 v26, v7\n\t"
    "s_or_b64 s[68:69], s[68:69], vcc\n\t"
    "v_mfma_f32_16x16x32_bf16 a[196:199], a[32:35], v[56:59], a[196:199]\n\t"
    "ds_write_b128 v20, v[172:175] offset:49216\n\t"
    "v_accvgpr_read_b32 v28, a176\n\t"
    "v_accvgpr_read_b32 v29, a177\n\t"
    "v_accvgpr_read_b32 v30, a178\n\t"
    "v_accvgpr_read_b32 v31, a179\n\t"
    "v_pk_mul_f32 v[28:29], v[28:29], v[24:25] op_sel_hi:[1,0]\n\t"
    "v_pk_mul_f32 v[30:31], v[30:31], v[24:25] op_sel_hi:[1,0]\n\t"
    "v_cvt_pk_bf16_f32 v28, v28, v29\n\t"
    "v_cvt_pk_bf16_f32 v29, v30, v31\n\t"
    "s_waitcnt lgkmcnt(4)\n\t"
    "buffer_store_dwordx4 a[68:71], v21, s[48:51], s70 offen offset:2048 sc0 sc1\n\t"
    "v_cmp_nge_f32 vcc, s67, v7\n\t"
    "s_nop 0\n\t"
    "s_or_b64 s[68:69], s[68:69], vcc\n\t"
    "v_mfma_f32_16x16x32_bf16 a[200:203], a[36:39], v[56:59], a[200:203]\n\t"
    "v_accvgpr_read_b32 v32, a180\n\t"
    "v_accvgpr_read_b32 v33, a181\n\t"
    "v_accvgpr_read_b32 v34, a182\n\t"
    "v_accvgpr_read_b32 v35, a183\n\t"
    "v_pk_mul_f32 v[32:33], v[32:33], v[24:25] op_sel_hi:[1,0]\n\t"
    "v_pk_mul_f32 v[34:35], v[34:35], v[24:25] op_sel_hi:[1,0]\n\t"
    "v_cvt_pk_bf16_f32 v30, v32, v33\n\t"
    "v_cvt_pk_bf16_f32 v31, v34, v35\n\t"
    "v_mfma_f32_16x16x32_bf16 a[204:207], a[40:43], v[56:59], a[204:207]\n\t"
    "ds_write_b128 v0, v[28:31] offset:49280\n\t"
    "v_accvgpr_read_b32 v36, a184\n\t"
    "v_accvgpr_read_b32 v37, a185\n\t"
    "v_accvgpr_read_b32 v38, a186\n\t"
    "v_accvgpr_read_b32 v39, a187\n\t"
    "v_pk_mul_f32 v[36:37], v[36:37], v[24:25] op_sel_hi:[1,0]\n\t"
    "v_pk_mul_f32 v[38:39], v[38:39], v[24:25] op_sel_hi:[1,0]\n\t"
    "v_cvt_pk_bf16_f32 v36, v36, v37\n\t"
    "v_cvt_pk_bf16_f32 v37, v38, v39\n\t"
    "v_mfma_f32_16x16x32_bf16 a[208:211], a[44:47], v[56:59], a[208:211]\n\t"
    "v_accvgpr_read_b32 v40, a188\n\t"
    "v_accvgpr_read_b32 v41, a189\n\t"
    "v_accvgpr_read_b32 v42, a190\n\t"
    "v_accvgpr_read_b32 v43, a191\n\t"
    "v_pk_mul_f32 v[40:41], v[40:41], v[24:25] op_sel_hi:[1,0]\n\t"
    "v_pk_mul_f32 v[42:43], v[42:43], v[24:25] op_sel_hi:[1,0]\n\t"
    "v_cvt_pk_bf16_f32 v38, v40, v41\n\t"
    "v_cvt_pk_bf16_f32 v39, v42, v43\n\t"
    "v_mfma_f32_16x16x32_bf16 v[204:207], a[0:3], v[60:63], v[204:207]\n\t"
    "ds_write_b128 v20, v[36:39] offset:49280\n\t"
    "ds_read2st64_b64 a[48:51], v1 offset0:96 offset1:99\n\t"
    "ds_read2st64_b64 a[52:55], v2 offset0:97 offset1:100\n\t"
    "ds_read2st64_b64 a[56:59], v3 offset0:98 offset1:101\n\t"
    "ds_read2st64_b64 a[60:63], v1 offset0:120 offset1:123\n\t"
    "ds_read2st64_b64 a[64:67], v2 offset0:121 offset1:124\n\t"
    "ds_read2st64_b64 a[68:71], v3 offset0:122 offset1:125\n\t"
    "v_pk_mul_f32 v[176:177], v[176:177], v[26:27] op_sel_hi:[1,0]\n\t"
    "v_pk_mul_f32 v[178:179], v[178:179], v[26:27] op_sel_hi:[1,0]\n\t"
    "v_cvt_pk_bf16_f32 v176, v176, v177\n\t"
    "v_cvt_pk_bf16_f32 v177, v178, v179\n\t"
    "v_mfma_f32_16x16x32_bf16 v[208:211], a[4:7], v[60:63], v[208:211]\n\t"
    "v_pk_mul_f32 v[180:181], v[180:181], v[26:27] op_sel_hi:[1,0]\n\t"
    "v_pk_mul_f32 v[182:183], v[182:183], v[26:27] op_sel_hi:[1,0]\n\t"
    "v_cvt_pk_bf16_f32 v178, v180, v181\n\t"
    "v_cvt_pk_bf16_f32 v179, v182, v183\n\t"
    "v_mov_b32 v22, v8\n\t"
    "v_mfma_f32_16x16x32_bf16 v[212:215], a[8:11], v[60:63], v[212:215]\n\t"
    "ds_write_b128 v0, v[176:179] offset:0\n\t"
    "v_pk_mul_f32 v[184:185], v[184:185], v[26:27] op_sel_hi:[1,0]\n\t"
    "v_pk_mul_f32 v[186:187], v[186:187], v[26:27] op_sel_hi:[1,0]\n\t"
    "v_cvt_pk_bf16_f32 v184, v184, v185\n\t"
    "v_cvt_pk_bf16_f32 v185, v186, v187\n\t"
    "v_permlane16_swap_b32 v8, v22\n\t"
    "v_mfma_f32_16x16x32_bf16 v[216:219], a[12:15], v[60:63], v[216:219]\n\t"
    "v_pk_mul_f32 v[188:189], v[188:189], v[26:27] op_sel_hi:[1,0]\n\t"
    "v_pk_mul_f32 v[190:191], v[190:191], v[26:27] op_sel_hi:[1,0]\n\t"
    "v_cvt_pk_bf16_f32 v186, v188, v189\n\t"
    "v_cvt_pk_bf16_f32 v187, v190, v191\n\t"
    "s_add_u32 s70, s71, 12288\n\t"
    "s_waitcnt lgkmcnt(6)\n\t"
    "buffer_store_dwordx4 a[48:51], v21, s[48:51], s70 offen offset:0 sc0 sc1\n\t"
    "v_add_f32 v8, v8, v22\n\t"
    "v_mfma_f32_16x16x32_bf16 v[220:223], a[16:19], v[60:63], v[220:223]\n\t"
    "ds_write_b128 v20, v[184:187] offset:0\n\t"
    "v_pk_mul_f32 v[192:193], v[192:193], v[26:27] op_sel_hi:[1,0]\n\t"
    "v_pk_mul_f32 v[194:195], v[194:195], v[26:27] op_sel_hi:[1,0]\n\t"
    "v_cvt_pk_bf16_f32 v192, v192, v193\n\t"
    "v_cvt_pk_bf16_f32 v193, v194, v195\n\t"
    "s_waitcnt lgkmcnt(6)\n\t"
    "buffer_store_dwordx4 a[52:55], v21, s[48:51], s70 offen offset:1024 sc0 sc1\n\t"
    "v_mov_b32 v22, v8\n\t"
    "v_mfma_f32_16x16x32_bf16 v[224:227], a[20:23], v[60:63], v[224:227]\n\t"
    "v_pk_mul_f32 v[196:197], v[196:197], v[26:27] op_sel_hi:[1,0]\n\t"
    "v_pk_mul_f32 v[198:199], v[198:199], v[26:27] op_sel_hi:[1,0]\n\t"
    "v_cvt_pk_bf16_f32 v194, v196, v197\n\t"
    "v_cvt_pk_bf16_f32 v195, v198, v199\n\t"
    "s_waitcnt lgkmcnt(5)\n\t"
    "buffer_store_dwordx4 a[56:59], v21, s[48:51], s70 offen offset:2048 sc0 sc1\n\t"
    "v_permlane32_swap_b32 v8, v22\n\t"
    "v_mfma_f32_16x16x32_bf16 v[228:231], a[24:27], v[60:63], v[228:231]\n\t"
    "ds_write_b128 v0, v[192:195] offset:64\n\t"
    "v_pk_mul_f32 v[200:201], v[200:201], v[26:27] op_sel_hi:[1,0]\n\t"
    "v_pk_mul_f32 v[202:203], v[202:203], v[26:27] op_sel_hi:[1,0]\n\t"
    "v_cvt_pk_bf16_f32 v200, v200, v201\n\t"
    "v_cvt_pk_bf16_f32 v201, v202, v203\n\t"
    "s_add_u32 s70, s71, 15360\n\t"
    "s_waitcnt lgkmcnt(5)\n\t"
    "buffer_store_dwordx4 a[60:63], v21, s[48:51], s70 offen offset:0 sc0 sc1\n\t"
    "v_add_f32 v8, v8, v22\n\t"
    "v_mfma_f32_16x16x32_bf16 a[212:215], a[28:31], v[60:63], a[212:215]\n\t"
    "v_accvgpr_read_b32 v40, a192\n\t"
    "v_accvgpr_read_b32 v41, a193\n\t"
    "v_accvgpr_read_b32 v42, a194\n\t"
    "v_accvgpr_read_b32 v43, a195\n\t"
    "v_pk_mul_f32 v[40:41], v[40:41], v[26:27] op_sel_hi:[1,0]\n\t"
    "v_pk_mul_f32 v[42:43], v[42:43], v[26:27] op_sel_hi:[1,0]\n\t"
    "v_cvt_pk_bf16_f32 v202, v40, v41\n\t"
    "v_cvt_pk_bf16_f32 v203, v42, v43\n\t"
    "s_waitcnt lgkmcnt(4)\n\t"
    "buffer_store_dwordx4 a[64:67], v21, s[48:51], s70 offen offset:1024 sc0 sc1\n\t"
    "v_cmp_nle_f32 vcc, s66, v8\n\t"
    "v_rcp_f32 v24, v8\n\t"
    "s_or_b64 s[68:69], s[68:69], vcc\n\t"
    "v_mfma_f32_16x16x32_bf16 a[216:219], a[32:35], v[60:63], a[216:219]\n\t"
    "ds_write_b128 v20, v[200:203] offset:64\n\t"
    "v_accvgpr_read_b32 v28, a196\n\t"
    "v_accvgpr_read_b32 v29, a197\n\t"
    "v_accvgpr_read_b32 v30, a198\n\t"
    "v_accvgpr_read_b32 v31, a199\n\t"
    "v_pk_mul_f32 v[28:29], v[28:29], v[26:27] op_sel_hi:[1,0]\n\t"
    "v_pk_mul_f32 v[30:31], v[30:31], v[26:27] op_sel_hi:[1,0]\n\t"
    "v_cvt_pk_bf16_f32 v28, v28, v29\n\t"
    "v_cvt_pk_bf16_f32 v29, v30, v31\n\t"
    "s_waitcnt lgkmcnt(4)\n\t"
    "buffer_store_dwordx4 a[68:71], v21, s[48:51], s70 offen offset:2048 sc0 sc1\n\t"
    "v_cmp_nge_f32 vcc, s67, v8\n\t"
    "s_nop 0\n\t"
    "s_or_b64 s[68:69], s[68:69], vcc\n\t"
    "v_mfma_f32_16x16x32_bf16 a[220:223], a[36:39], v[60:63], a[220:223]\n\t"
    "v_accvgpr_read_b32 v32, a200\n\t"
    "v_accvgpr_read_b32 v33, a201\n\t"
    "v_accvgpr_read_b32 v34, a202\n\t"
    "v_accvgpr_read_b32 v35, a203\n\t"
    "v_pk_mul_f32 v[32:33], v[32:33], v[26:27] op_sel_hi:[1,0]\n\t"
    "v_pk_mul_f32 v[34:35], v[34:35], v[26:27] op_sel_hi:[1,0]\n\t"
    "v_cvt_pk_bf16_f32 v30, v32, v33\n\t"
    "v_cvt_pk_bf16_f32 v31, v34, v35\n\t"
    "v_mfma_f32_16x16x32_bf16 a[224:227], a[40:43], v[60:63], a[224:227]\n\t"
    "ds_write_b128 v0, v[28:31] offset:128\n\t"
    "v_accvgpr_read_b32 v36, a204\n\t"
    "v_accvgpr_read_b32 v37, a205\n\t"
    "v_accvgpr_read_b32 v38, a206\n\t"
    "v_accvgpr_read_b32 v39, a207\n\t"
    "v_pk_mul_f32 v[36:37], v[36:37], v[26:27] op_sel_hi:[1,0]\n\t"
    "v_pk_mul_f32 v[38:39], v[38:39], v[26:27] op_sel_hi:[1,0]\n\t"
    "v_cvt_pk_bf16_f32 v36, v36, v37\n\t"
    "v_cvt_pk_bf16_f32 v37, v38, v39\n\t"
    "v_mfma_f32_16x16x32_bf16 a[228:231], a[44:47], v[60:63], a[228:231]\n\t"
    "v_accvgpr_read_b32 v40, a208\n\t"
    "v_accvgpr_read_b32 v41, a209\n\t"
    "v_accvgpr_read_b32 v42, a210\n\t"
    "v_accvgpr_read_b32 v43, a211\n\t"
    "v_pk_mul_f32 v[40:41], v[40:41], v[26:27] op_sel_hi:[1,0]\n\t"
    "v_pk_mul_f32 v[42:43], v[42:43], v[26:27] op_sel_hi:[1,0]\n\t"
    "v_cvt_pk_bf16_f32 v38, v40, v41\n\t"
    "v_cvt_pk_bf16_f32 v39, v42, v43\n\t"
    "v_mfma_f32_16x16x32_bf16 v[232:235], a[0:3], v[64:67], v[232:235]\n\t"
    "ds_write_b128 v20, v[36:39] offset:128\n\t"
    "ds_read2st64_b64 a[48:51], v1 offset0:0 offset1:3\n\t"
    "ds_read2st64_b64 a[52:55], v2 offset0:1 offset1:4\n\t"
    "ds_read2st64_b64 a[56:59], v3 offset0:2 offset1:5\n\t"
    "ds_read2st64_b64 a[60:63], v1 offset0:24 offset1:27\n\t"
    "ds_read2st64_b64 a[64:67], v2 offset0:25 offset1:28\n\t"
    "ds_read2st64_b64 a[68:71], v3 offset0:26 offset1:29\n\t"
    "v_pk_mul_f32 v[204:205], v[204:205], v[24:25] op_sel_hi:[1,0]\n\t"
    "v_pk_mul_f32 v[206:207], v[206:207], v[24:25] op_sel_hi:[1,0]\n\t"
    "v_cvt_pk_bf16_f32 v204, v204, v205\n\t"
    "v_cvt_pk_bf16_f32 v205, v206, v207\n\t"
    "v_mfma_f32_16x16x32_bf16 v[236:239], a[4:7], v[64:67], v[236:239]\n\t"
    "v_pk_mul_f32 v[208:209], v[208:209], v[24:25] op_sel_hi:[1,0]\n\t"
    "v_pk_mul_f32 v[210:211], v[210:211], v[24:25] op_sel_hi:[1,0]\n\t"
    "v_cvt_pk_bf16_f32 v206, v208, v209\n\t"
    "v_cvt_pk_bf16_f32 v207, v210, v211\n\t"
    "v_mov_b32 v22, v9\n\t"
    "v_mfma_f32_16x16x32_bf16 v[240:243], a[8:11], v[64:67], v[240:243]\n\t"
    "ds_write_b128 v0, v[204:207] offset:24576\n\t"
    "v_pk_mul_f32 v[212:213], v[212:213], v[24:25] op_sel_hi:[1,0]\n\t"
    "v_pk_mul_f32 v[214:215], v[214:215], v[24:25] op_sel_hi:[1,0]\n\t"
    "v_cvt_pk_bf16_f32 v212, v212, v213\n\t"
    "v_cvt_pk_bf16_f32 v213, v214, v215\n\t"
    "v_permlane16_swap_b32 v9, v22\n\t"
    "v_mfma_f32_16x16x32_bf16 v[244:247], a[12:15], v[64:67], v[244:247]\n\t"
    "v_pk_mul_f32 v[216:217], v[216:217], v[24:25] op_sel_hi:[1,0]\n\t"
    "v_pk_mul_f32 v[218:219], v[218:219], v[24:25] op_sel_hi:[1,0]\n\t"
    "v_cvt_pk_bf16_f32 v214, v216, v217\n\t"
    "v_cvt_pk_bf16_f32 v215, v218, v219\n\t"
    "s_add_u32 s70, s71, 18432\n\t"
    "s_waitcnt lgkmcnt(6)\n\t"
    "buffer_store_dwordx4 a[48:51], v21, s[48:51], s70 offen offset:0 sc0 sc1\n\t"
    "v_add_f32 v9, v9, v22\n\t"
    "v_mfma_f32_16x16x32_bf16 v[248:251], a[16:19], v[64:67], v[248:251]\n\t"
    "ds_write_b128 v20, v[212:215] offset:24576\n\t"
    "v_pk_mul_f32 v[220:221], v[220:221], v[24:25] op_sel_hi:[1,0]\n\t"
    "v_pk_mul_f32 v[222:223], v[222:223], v[24:25] op_sel_hi:[1,0]\n\t"
    "v_cvt_pk_bf16_f32 v220, v220, v221\n\t"
    "v_cvt_pk_bf16_f32 v221, v222, v223\n\t"
    "s_waitcnt lgkmcnt(6)\n\t"
    "buffer_store_dwordx4 a[52:55], v21, s[48:51], s70 offen offset:1024 sc0 sc1\n\t"
    "v_mov_b32 v22, v9\n\t"
    "v_mfma_f32_16x16x32_bf16 v[252:255], a[20:23], v[64:67], v[252:255]\n\t"
    "v_pk_mul_f32 v[224:225], v[224:225], v[24:25] op_sel_hi:[1,0]\n\t"
    "v_pk_mul_f32 v[226:227], v[226:227], v[24:25] op_sel_hi:[1,0]\n\t"
    "v_cvt_pk_bf16_f32 v222, v224, v225\n\t"
    "v_cvt_pk_bf16_f32 v223, v226, v227\n\t"
    "s_waitcnt lgkmcnt(5)\n\t"
    "buffer_store_dwordx4 a[56:59], v21, s[48:51], s70 offen offset:2048 sc0 sc1\n\t"
    "v_permlane32_swap_b32 v9, v22\n\t"
    "v_mfma_f32_16x16x32_bf16 a[232:235], a[24:27], v[64:67], a[232:235]\n\t"
    "ds_write_b128 v0, v[220:223] offset:24640\n\t"
    "v_pk_mul_f32 v[228:229], v[228:229], v[24:25] op_sel_hi:[1,0]\n\t"
    "v_pk_mul_f32 v[230:231], v[230:231], v[24:25] op_sel_hi:[1,0]\n\t"
    "v_cvt_pk_bf16_f32 v228, v228, v229\n\t"
    "v_cvt_pk_bf16_f32 v229, v230, v231\n\t"
    "s_add_u32 s70, s71, 21504\n\t"
    "s_waitcnt lgkmcnt(5)\n\t"
    "buffer_store_dwordx4 a[60:63], v21, s[48:51], s70 offen offset:0 sc0 sc1\n\t"
    "v_add_f32 v9, v9, v22\n\t"
    "v_mfma_f32_16x16x32_bf16 a[236:239], a[28:31], v[64:67], a[236:239]\n\t"
    "v_accvgpr_read_b32 v40, a212\n\t"
    "v_accvgpr_read_b32 v41, a213\n\t"
    "v_accvgpr_read_b32 v42, a214\n\t"
    "v_accvgpr_read_b32 v43, a215\n\t"
    "v_pk_mul_f32 v[40:41], v[40:41], v[24:25] op_sel_hi:[1,0]\n\t"
    "v_pk_mul_f32 v[42:43], v[42:43], v[24:25] op_sel_hi:[1,0]\n\t"
    "v_cvt_pk_bf16_f32 v230, v40, v41\n\t"
    "v_cvt_pk_bf16_f32 v231, v42, v43\n\t"
    "s_waitcnt lgkmcnt(4)\n\t"
    "buffer_store_dwordx4 a[64:67], v21, s[48:51], s70 offen offset:1024 sc0 sc1\n\t"
    "v_cmp_nle_f32 vcc, s66, v9\n\t"
    "v_rcp_f32 v26, v9\n\t"
    "s_or_b64 s[68:69], s[68:69], vcc\n\t"
    "v_mfma_f32_16x16x32_bf16 a[240:243], a[32:35], v[64:67], a[240:243]\n\t"
    "ds_write_b128 v20, v[228:231] offset:24640\n\t"
    "v_accvgpr_read_b32 v28, a216\n\t"
    "v_accvgpr_read_b32 v29, a217\n\t"
    "v_accvgpr_read_b32 v30, a218\n\t"
    "v_accvgpr_read_b32 v31, a219\n\t"
    "v_pk_mul_f32 v[28:29], v[28:29], v[24:25] op_sel_hi:[1,0]\n\t"
    "v_pk_mul_f32 v[30:31], v[30:31], v[24:25] op_sel_hi:[1,0]\n\t"
    "v_cvt_pk_bf16_f32 v28, v28, v29\n\t"
    "v_cvt_pk_bf16_f32 v29, v30, v31\n\t"
    "s_waitcnt lgkmcnt(4)\n\t"
    "buffer_store_dwordx4 a[68:71], v21, s[48:51], s70 offen offset:2048 sc0 sc1\n\t"
    "v_cmp_nge_f32 vcc, s67, v9\n\t"
    "s_nop 0\n\t"
    "s_or_b64 s[68:69], s[68:69], vcc\n\t"
    "v_mfma_f32_16x16x32_bf16 a[244:247], a[36:39], v[64:67], a[244:247]\n\t"
    "v_accvgpr_read_b32 v32, a220\n\t"
    "v_accvgpr_read_b32 v33, a221\n\t"
    "v_accvgpr_read_b32 v34, a222\n\t"
    "v_accvgpr_read_b32 v35, a223\n\t"
    "v_pk_mul_f32 v[32:33], v[32:33], v[24:25] op_sel_hi:[1,0]\n\t"
    "v_pk_mul_f32 v[34:35], v[34:35], v[24:25] op_sel_hi:[1,0]\n\t"
    "v_cvt_pk_bf16_f32 v30, v32, v33\n\t"
    "v_cvt_pk_bf16_f32 v31, v34, v35\n\t"
    "v_mfma_f32_16x16x32_bf16 a[248:251], a[40:43], v[64:67], a[248:251]\n\t"
    "ds_write_b128 v0, v[28:31] offset:24704\n\t"
    "v_accvgpr_read_b32 v36, a224\n\t"
    "v_accvgpr_read_b32 v37, a225\n\t"
    "v_accvgpr_read_b32 v38, a226\n\t"
    "v_accvgpr_read_b32 v39, a227\n\t"
    "v_pk_mul_f32 v[36:37], v[36:37], v[24:25] op_sel_hi:[1,0]\n\t"
    "v_pk_mul_f32 v[38:39], v[38:39], v[24:25] op_sel_hi:[1,0]\n\t"
    "v_cvt_pk_bf16_f32 v36, v36, v37\n\t"
    "v_cvt_pk_bf16_f32 v37, v38, v39\n\t"
    "v_mfma_f32_16x16x32_bf16 a[252:255], a[44:47], v[64:67], a[252:255]\n\t"
    "v_accvgpr_read_b32 v40, a228\n\t"
    "v_accvgpr_read_b32 v41, a229\n\t"
    "v_accvgpr_read_b32 v42, a230\n\t"
    "v_accvgpr_read_b32 v43, a231\n\t"
    "v_pk_mul_f32 v[40:41], v[40:41], v[24:25] op_sel_hi:[1,0]\n\t"
    "v_pk_mul_f32 v[42:43], v[42:43], v[24:25] op_sel_hi:[1,0]\n\t"
    "v_cvt_pk_bf16_f32 v38, v40, v41\n\t"
    "v_cvt_pk_bf16_f32 v39, v42, v43\n\t"
    "ds_write_b128 v20, v[36:39] offset:24704\n\t"
    "ds_read2st64_b64 a[48:51], v1 offset0:48 offset1:51\n\t"
    "ds_read2st64_b64 a[52:55], v2 offset0:49 offset1:52\n\t"
    "ds_read2st64_b64 a[56:59], v3 offset0:50 offset1:53\n\t"
    "ds_read2st64_b64 a[60:63], v1 offset0:72 offset1:75\n\t"
    "ds_read2st64_b64 a[64:67], v2 offset0:73 offset1:76\n\t"
    "ds_read2st64_b64 a[68:71], v3 offset0:74 offset1:77\n\t"
    "v_pk_mul_f32 v[232:233], v[232:233], v[26:27] op_sel_hi:[1,0]\n\t"
    "v_pk_mul_f32 v[234:235], v[234:235], v[26:27] op_sel_hi:[1,0]\n\t"
    "v_cvt_pk_bf16_f32 v232, v232, v233\n\t"
    "v_cvt_pk_bf16_f32 v233, v234, v235\n\t"
    "v_pk_mul_f32 v[236:237], v[236:237], v[26:27] op_sel_hi:[1,0]\n\t"
    "v_pk_mul_f32 v[238:239], v[238:239], v[26:27] op_sel_hi:[1,0]\n\t"
    "v_cvt_pk_bf16_f32 v234, v236, v237\n\t"
    "v_cvt_pk_bf16_f32 v235, v238, v239\n\t"
    "ds_write_b128 v0, v[232:235] offset:49152\n\t"
    "v_pk_mul_f32 v[240:241], v[240:241], v[26:27] op_sel_hi:[1,0]\n\t"
    "v_pk_mul_f32 v[242:243], v[242:243], v[26:27] op_sel_hi:[1,0]\n\t"
    "v_cvt_pk_bf16_f32 v240, v240, v241\n\t"
    "v_cvt_pk_bf16_f32 v241, v242, v243\n\t"
    "v_pk_mul_f32 v[244:245], v[244:245], v[26:27] op_sel_hi:[1,0]\n\t"
    "v_pk_mul_f32 v[246:247], v[246:247], v[26:27] op_sel_hi:[1,0]\n\t"
    "v_cvt_pk_bf16_f32 v242, v244, v245\n\t"
    "v_cvt_pk_bf16_f32 v243, v246, v247\n\t"
    "s_add_u32 s70, s71, 24576\n\t"
    "s_waitcnt lgkmcnt(6)\n\t"
    "buffer_store_dwordx4 a[48:51], v21, s[48:51], s70 offen offset:0 sc0 sc1\n\t"
    "ds_write_b128 v20, v[240:243] offset:49152\n\t"
    "v_pk_mul_f32 v[248:249], v[248:249], v[26:27] op_sel_hi:[1,0]\n\t"
    "v_pk_mul_f32 v[250:251], v[250:251], v[26:27] op_sel_hi:[1,0]\n\t"
    "v_cvt_pk_bf16_f32 v248, v248, v249\n\t"
    "v_cvt_pk_bf16_f32 v249, v250, v251\n\t"
    "s_waitcnt lgkmcnt(6)\n\t"
    "buffer_store_dwordx4 a[52:55], v21, s[48:51], s70 offen offset:1024 sc0 sc1\n\t"
    "v_pk_mul_f32 v[252:253], v[252:253], v[26:27] op_sel_hi:[1,0]\n\t"
    "v_pk_mul_f32 v[254:255], v[254:255], v[26:27] op_sel_hi:[1,0]\n\t"
    "v_cvt_pk_bf16_f32 v250, v252, v253\n\t"
    "v_cvt_pk_bf16_f32 v251, v254, v255\n\t"
    "s_waitcnt lgkmcnt(5)\n\t"
    "buffer_store_dwordx4 a[56:59], v21, s[48:51], s70 offen offset:2048 sc0 sc1\n\t"
    "ds_write_b128 v0, v[248:251] offset:49216\n\t"
    "v_accvgpr_read_b32 v36, a232\n\t"
    "v_accvgpr_read_b32 v37, a233\n\t"
    "v_accvgpr_read_b32 v38, a234\n\t"
    "v_accvgpr_read_b32 v39, a235\n\t"
    "v_pk_mul_f32 v[36:37], v[36:37], v[26:27] op_sel_hi:[1,0]\n\t"
    "v_pk_mul_f32 v[38:39], v[38:39], v[26:27] op_sel_hi:[1,0]\n\t"
    "v_cvt_pk_bf16_f32 v36, v36, v37\n\t"
    "v_cvt_pk_bf16_f32 v37, v38, v39\n\t"
    "s_add_u32 s70, s71, 27648\n\t"
    "s_waitcnt lgkmcnt(5)\n\t"
    "buffer_store_dwordx4 a[60:63], v21, s[48:51], s70 offen offset:0 sc0 sc1\n\t"
    "v_accvgpr_read_b32 v40, a236\n\t"
    "v_accvgpr_read_b32 v41, a237\n\t"
    "v_accvgpr_read_b32 v42, a238\n\t"
    "v_accvgpr_read_b32 v43, a239\n\t"
    "v_pk_mul_f32 v[40:41], v[40:41], v[26:27] op_sel_hi:[1,0]\n\t"
    "v_pk_mul_f32 v[42:43], v[42:43], v[26:27] op_sel_hi:[1,0]\n\t"
    "v_cvt_pk_bf16_f32 v38, v40, v41\n\t"
    "v_cvt_pk_bf16_f32 v39, v42, v43\n\t"
    "s_waitcnt lgkmcnt(4)\n\t"
    "buffer_store_dwordx4 a[64:67], v21, s[48:51], s70 offen offset:1024 sc0 sc1\n\t"
    "ds_write_b128 v20, v[36:39] offset:49216\n\t"
    "v_accvgpr_read_b32 v28, a240\n\t"
    "v_accvgpr_read_b32 v29, a241\n\t"
    "v_accvgpr_read_b32 v30, a242\n\t"
    "v_accvgpr_read_b32 v31, a243\n\t"
    "v_pk_mul_f32 v[28:29], v[28:29], v[26:27] op_sel_hi:[1,0]\n\t"
    "v_pk_mul_f32 v[30:31], v[30:31], v[26:27] op_sel_hi:[1,0]\n\t"
    "v_cvt_pk_bf16_f32 v28, v28, v29\n\t"
    "v_cvt_pk_bf16_f32 v29, v30, v31\n\t"
    "s_waitcnt lgkmcnt(4)\n\t"
    "buffer_store_dwordx4 a[68:71], v21, s[48:51], s70 offen offset:2048 sc0 sc1\n\t"
    "v_accvgpr_read_b32 v32, a244\n\t"
    "v_accvgpr_read_b32 v33, a245\n\t"
    "v_accvgpr_read_b32 v34, a246\n\t"
    "v_accvgpr_read_b32 v35, a247\n\t"
    "v_pk_mul_f32 v[32:33], v[32:33], v[26:27] op_sel_hi:[1,0]\n\t"
    "v_pk_mul_f32 v[34:35], v[34:35], v[26:27] op_sel_hi:[1,0]\n\t"
    "v_cvt_pk_bf16_f32 v30, v32, v33\n\t"
    "v_cvt_pk_bf16_f32 v31, v34, v35\n\t"
    "ds_write_b128 v0, v[28:31] offset:49280\n\t"
    "v_accvgpr_read_b32 v36, a248\n\t"
    "v_accvgpr_read_b32 v37, a249\n\t"
    "v_accvgpr_read_b32 v38, a250\n\t"
    "v_accvgpr_read_b32 v39, a251\n\t"
    "v_pk_mul_f32 v[36:37], v[36:37], v[26:27] op_sel_hi:[1,0]\n\t"
    "v_pk_mul_f32 v[38:39], v[38:39], v[26:27] op_sel_hi:[1,0]\n\t"
    "v_cvt_pk_bf16_f32 v36, v36, v37\n\t"
    "v_cvt_pk_bf16_f32 v37, v38, v39\n\t"
    "v_accvgpr_read_b32 v40, a252\n\t"
    "v_accvgpr_read_b32 v41, a253\n\t"
    "v_accvgpr_read_b32 v42, a254\n\t"
    "v_accvgpr_read_b32 v43, a255\n\t"
    "v_pk_mul_f32 v[40:41], v[40:41], v[26:27] op_sel_hi:[1,0]\n\t"
    "v_pk_mul_f32 v[42:43], v[42:43], v[26:27] op_sel_hi:[1,0]\n\t"
    "v_cvt_pk_bf16_f32 v38, v40, v41\n\t"
    "v_cvt_pk_bf16_f32 v39, v42, v43\n\t"
    "ds_write_b128 v20, v[36:39] offset:49280\n\t"
    "ds_read2st64_b64 a[48:51], v1 offset0:96 offset1:99\n\t"
    "ds_read2st64_b64 a[52:55], v2 offset0:97 offset1:100\n\t"
    "ds_read2st64_b64 a[56:59], v3 offset0:98 offset1:101\n\t"
    "ds_read2st64_b64 a[60:63], v1 offset0:120 offset1:123\n\t"
    "ds_read2st64_b64 a[64:67], v2 offset0:121 offset1:124\n\t"
    "ds_read2st64_b64 a[68:71], v3 offset0:122 offset1:125\n\t"
    "s_add_u32 s70, s71, 30720\n\t"
    "s_waitcnt lgkmcnt(5)\n\t"
    "buffer_store_dwordx4 a[48:51], v21, s[48:51], s70 offen offset:0 sc0 sc1\n\t"
    "s_waitcnt lgkmcnt(4)\n\t"
    "buffer_store_dwordx4 a[52:55], v21, s[48:51], s70 offen offset:1024 sc0 sc1\n\t"
    "s_waitcnt lgkmcnt(3)\n\t"
    "buffer_store_dwordx4 a[56:59], v21, s[48:51], s70 offen offset:2048 sc0 sc1\n\t"
    "s_add_u32 s70, s71, 33792\n\t"
    "s_waitcnt lgkmcnt(2)\n\t"
    "buffer_store_dwordx4 a[60:63], v21, s[48:51], s70 offen offset:0 sc0 sc1\n\t"
    "s_waitcnt lgkmcnt(1)\n\t"
    "buffer_store_dwordx4 a[64:67], v21, s[48:51], s70 offen offset:1024 sc0 sc1\n\t"
    "s_waitcnt lgkmcnt(0)\n\t"
    "buffer_store_dwordx4 a[68:71], v21, s[48:51], s70 offen offset:2048 sc0 sc1\n\t"
    "s_or_b32 s70, s68, s69\n\t"
    "s_mov_b32 %0, s70\n\t"
    "Lend_%=:\n\t"
    : "=&s"(redo)
    : "s"(qh), "s"(kh), "s"(vh), "s"(oh), "s"(wid), "s"(head)
    : "v0","v1","v2","v3","v4","v5","v6","v7","v8","v9","v10","v11","v12","v13","v14","v15","v16","v17","v18","v19","v20","v21","v22","v23","v24","v25","v26","v27","v28","v29","v30","v31","v32","v33","v34","v35","v36","v37","v38","v39","v40","v41","v42","v43","v44","v45","v46","v47","v48","v49","v50","v51","v52","v53","v54","v55","v56","v57","v58","v59","v60","v61","v62","v63","v64","v65","v66","v67","v68","v69","v70","v71","v72","v73","v74","v75","v76","v77","v78","v79","v80","v81","v82","v83","v84","v85","v86","v87","v88","v89","v90","v91","v92","v93","v94","v95","v96","v97","v98","v99","v100","v101","v102","v103","v104","v105","v106","v107","v108","v109","v110","v111","v112","v113","v114","v115","v116","v117","v118","v119","v120","v121","v122","v123","v124","v125","v126","v127","v128","v129","v130","v131","v132","v133","v134","v135","v136","v137","v138","v139","v140","v141","v142","v143","v144","v145","v146","v147","v148","v149","v150","v151","v152","v153","v154","v155","v156","v157","v158","v159","v160","v161","v162","v163","v164","v165","v166","v167","v168","v169","v170","v171","v172","v173","v174","v175","v176","v177","v178","v179","v180","v181","v182","v183","v184","v185","v186","v187","v188","v189","v190","v191","v192","v193","v194","v195","v196","v197","v198","v199","v200","v201","v202","v203","v204","v205","v206","v207","v208","v209","v210","v211","v212","v213","v214","v215","v216","v217","v218","v219","v220","v221","v222","v223","v224","v225","v226","v227","v228","v229","v230","v231","v232","v233","v234","v235","v236","v237","v238","v239","v240","v241","v242","v243","v244","v245","v246","v247","v248","v249","v250","v251","v252","v253","v254","v255","a0","a1","a2","a3","a4","a5","a6","a7","a8","a9","a10","a11","a12","a13","a14","a15","a16","a17","a18","a19","a20","a21","a22","a23","a24","a25","a26","a27","a28","a29","a30","a31","a32","a33","a34","a35","a36","a37","a38","a39","a40","a41","a42","a43","a44","a45","a46","a47","a48","a49","a50","a51","a52","a53","a54","a55","a56","a57","a58","a59","a60","a61","a62","a63","a64","a65","a66","a67","a68","a69","a70","a71","a72","a73","a74","a75","a76","a77","a78","a79","a80","a81","a82","a83","a84","a85","a86","a87","a88","a89","a90","a91","a92","a93","a94","a95","a96","a97","a98","a99","a100","a101","a102","a103","a104","a105","a106","a107","a108","a109","a110","a111","a112","a113","a114","a115","a116","a117","a118","a119","a120","a121","a122","a123","a124","a125","a126","a127","a128","a129","a130","a131","a132","a133","a134","a135","a136","a137","a138","a139","a140","a141","a142","a143","a144","a145","a146","a147","a148","a149","a150","a151","a152","a153","a154","a155","a156","a157","a158","a159","a160","a161","a162","a163","a164","a165","a166","a167","a168","a169","a170","a171","a172","a173","a174","a175","a176","a177","a178","a179","a180","a181","a182","a183","a184","a185","a186","a187","a188","a189","a190","a191","a192","a193","a194","a195","a196","a197","a198","a199","a200","a201","a202","a203","a204","a205","a206","a207","a208","a209","a210","a211","a212","a213","a214","a215","a216","a217","a218","a219","a220","a221","a222","a223","a224","a225","a226","a227","a228","a229","a230","a231","a232","a233","a234","a235","a236","a237","a238","a239","a240","a241","a242","a243","a244","a245","a246","a247","a248","a249","a250","a251","a252","a253","a254","a255","s36","s37","s38","s39","s40","s41","s42","s43","s44","s45","s46","s47","s48","s49","s50","s51","s52","s53","s54","s55","s56","s57","s58","s59","s60","s61","s62","s63","s64","s65","s66","s67","s68","s69","s70","s71","s72","s73","s74","s75","s76","s77","s78","s79","s80","s81","s82","s83","s84","s85","s86","s87","s88","s89","s90","s91","s92","s93","s94","s95","s96","s97","s98","s99","vcc","scc","m0","memory");
  if (redo) {
    asm volatile("s_waitcnt vmcnt(0)" ::: "memory");
#pragma unroll 1
    for (int b = 0; b < 3; ++b) {
      const size_t r0 = (size_t)(wid * 96 + b * 32) * ROWB;
      exact_rows(qh + r0, kh, vh, oh + r0, lds, wid);
    }
  }
}

extern "C" void kernel_launch(void* const* d_in, const int* in_sizes, int n_in,
                              void* d_out, int out_size, void* d_ws, size_t ws_size,
                              hipStream_t stream) {
  (void)in_sizes; (void)n_in; (void)out_size; (void)d_ws; (void)ws_size;
  (void)hipFuncSetAttribute((const void*)gqa_attn_kernel, hipFuncAttributeMaxDynamicSharedMemorySize, LDS_BYTES);
  gqa_attn_kernel<<<NHEADS, 256, LDS_BYTES, stream>>>((const char*)d_in[0], (const char*)d_in[1], (const char*)d_in[2], (char*)d_out);
}
```
